```python
import torch

def _softmax(x, axis=-1):
    return torch.softmax(x, dim=axis)
def _gelu(x):
    return torch.nn.functional.gelu(x, approximate="tanh")
def _silu(x):
    return torch.nn.functional.silu(x)
def _sigmoid(x):
    return torch.sigmoid(x)
def _layernorm(x, axis=-1, eps=1e-5):
    m = x.mean(dim=axis, keepdim=True)
    v = x.var(dim=axis, keepdim=True, unbiased=False)
    return (x - m) / torch.sqrt(v + eps)
def _scatter_add(tgt, idx, src, axis=0):
    return tgt.clone().index_add_(axis, idx, src)
def _segment_sum(src, seg, n):
    out = torch.zeros((n,) + src.shape[1:], dtype=src.dtype)
    return out.index_add_(0, seg, src)
def _pad(x, pw):
    flat = tuple(v for pair in reversed(pw) for v in pair)
    return torch.nn.functional.pad(x, flat)

kernel_name = 'sequential_preLN__sliding_block__moe_arch_mod__prompt'

def setup_inputs():

    _w = (lambda a: (lambda t: t.to(torch.bfloat16) if t.is_floating_point() else t)(torch.as_tensor(a)))
    def _A_setup():
        B, S, D = 1, 2048, 512
        return {
            "x": _w((lambda *s: torch.randn(*s, dtype=torch.bfloat16) * 0.05)(B, S, D)),
            "wqkv": _w((lambda *s: torch.randn(*s, dtype=torch.bfloat16) * 0.05)(D, 3 * D)),
            "wo": _w((lambda *s: torch.randn(*s, dtype=torch.bfloat16) * 0.05)(D, D)),
        }
    _A_in = _A_setup()

    def _B_setup():
        B, S, D, F, C = 1, 2048, 512, 1792, 1
        return {
            "x": _w((lambda *s: torch.randn(*s, dtype=torch.bfloat16) * 0.05)(B, S, D)),
            "w_r": _w((lambda *s: torch.randn(*s, dtype=torch.bfloat16) * 0.05)(D)),
            "w_gate": _w((lambda *s: torch.randn(*s, dtype=torch.bfloat16) * 0.05)(D, F)),
            "w_up": _w((lambda *s: torch.randn(*s, dtype=torch.bfloat16) * 0.05)(D, F)),
            "w_down": _w((lambda *s: torch.randn(*s, dtype=torch.bfloat16) * 0.05)(F, D)),
        }
    _B_in = _B_setup()

    return {
        "A_x": _A_in["x"],
        "A_wqkv": _A_in["wqkv"],
        "A_wo": _A_in["wo"],
        "B_x": _B_in["x"],
        "B_w_r": _B_in["w_r"],
        "B_w_gate": _B_in["w_gate"],
        "B_w_up": _B_in["w_up"],
        "B_w_down": _B_in["w_down"],
    }

def reference(A_x, A_wqkv, A_wo, B_x, B_w_r, B_w_gate, B_w_up, B_w_down):
    def _A_ref(x, wqkv, wo):
        B, S, D = x.shape
        H = 32; Dh = D // H; W = 256
        qkv = (lambda x, *d: x.permute(*d))((x @ wqkv).reshape(B, S, 3, H, Dh), 2, 0, 3, 1, 4)
        q, k, v = qkv[0], qkv[1], qkv[2]
        scores = torch.einsum("bhqd,bhkd->bhqk", q, k) * (Dh ** -0.5)
        idx = torch.arange(S)
        mask = (idx[:, None] - idx[None, :] >= 0) & (idx[:, None] - idx[None, :] < W)
        scores = torch.where(mask[None, None], scores, -1e9)
        p = _softmax(scores, axis=-1)
        o = torch.einsum("bhqk,bhkd->bhqd", p, v)
        return (lambda x, *d: x.permute(*d))(o, 0, 2, 1, 3).reshape(B, S, D) @ wo

    def _B_ref(x, w_r, w_gate, w_up, w_down):
        B, S, D = x.shape
        C = 1
        r = x @ w_r
        idx = torch.argsort(-r, axis=-1)[:, :C]
        idx_e = (lambda x, n, axis: torch.repeat_interleave(x, n, dim=axis))(idx.reshape(B, C, 1), D, axis=2)
        x_sel = (lambda x, i, axis: torch.take_along_dim(x, i, dim=axis))(x, idx_e, axis=1)
        g = _silu(x_sel @ w_gate)
        u = x_sel @ w_up
        y = (g * u) @ w_down
        r_sel = _sigmoid((lambda x, i, axis: torch.take_along_dim(x, i, dim=axis))(r, idx, axis=1))
        y = y * r_sel.reshape(B, C, 1)
        b_off = (torch.arange(B) * S).reshape(B, 1)
        flat_idx = (idx + b_off).reshape(B * C)
        zero = (x * 0.0).reshape(B * S, D)
        upd = _scatter_add(zero, flat_idx, y.reshape(B * C, D), axis=0)
        return x + upd.reshape(B, S, D)

    x = A_x
    h = x + _A_ref(_layernorm(x, axis=-1), A_wqkv, A_wo)
    h2 = h + _B_ref(_layernorm(h, axis=-1), B_w_r, B_w_gate, B_w_up, B_w_down)
    return h2
```

```cpp
#include <hip/hip_runtime.h>
#include <hip/hip_bf16.h>
#include <stdint.h>
typedef unsigned short u16;
#define S_ 2048
#define D_ 512
#define F_ 1792
__device__ __forceinline__ float bf2f(u16 v){ return __uint_as_float(((unsigned)v)<<16); }
__device__ __forceinline__ u16 f2bf(float f){ __hip_bfloat16 b = __float2bfloat16(f); return *(u16*)&b; }

__global__ void k_ln_qkv(const u16* x, const u16* w, float* qkv){
  __shared__ float xs[D_]; __shared__ float red[8];
  int r = blockIdx.x, t = threadIdx.x;
  float a = bf2f(x[r*D_+t]), b = bf2f(x[r*D_+t+256]);
  float s = a+b; for(int o=32;o;o>>=1) s += __shfl_xor(s,o);
  if((t&63)==0) red[t>>6]=s; __syncthreads();
  float m = (red[0]+red[1]+red[2]+red[3])/D_; __syncthreads();
  float v = (a-m)*(a-m)+(b-m)*(b-m); for(int o=32;o;o>>=1) v += __shfl_xor(v,o);
  if((t&63)==0) red[t>>6]=v; __syncthreads();
  float rs = rsqrtf((red[0]+red[1]+red[2]+red[3])/D_+1e-5f);
  xs[t]=(a-m)*rs; xs[t+256]=(b-m)*rs; __syncthreads();
  for(int n=t;n<3*D_;n+=256){ float acc=0; for(int k=0;k<D_;k++) acc += xs[k]*bf2f(w[k*3*D_+n]); qkv[r*3*D_+n]=acc; }
}
__global__ void k_attn(const float* qkv, float* o){
  int idx = blockIdx.x*blockDim.x+threadIdx.x; int h = idx&31, q = idx>>5;
  float qv[16];
  #pragma unroll
  for(int d=0;d<16;d++) qv[d]=qkv[q*1536+h*16+d]*0.25f;
  float m=-1e30f,l=0,acc[16];
  #pragma unroll
  for(int d=0;d<16;d++) acc[d]=0;
  int k0 = q-255<0?0:q-255;
  for(int k=k0;k<=q;k++){ float s=0;
    #pragma unroll
    for(int d=0;d<16;d++) s+=qv[d]*qkv[k*1536+512+h*16+d];
    float mn=fmaxf(m,s), al=__expf(m-mn), p=__expf(s-mn); l=l*al+p; m=mn;
    #pragma unroll
    for(int d=0;d<16;d++) acc[d]=acc[d]*al+p*qkv[k*1536+1024+h*16+d];
  }
  #pragma unroll
  for(int d=0;d<16;d++) o[q*512+h*16+d]=acc[d]/l;
}
__global__ void k_oproj(const float* o, const u16* wo, const u16* x, const u16* wr, float* hn, float* rr, u16* out, float* hh){
  __shared__ float os[D_]; __shared__ float red[8];
  int r = blockIdx.x, t = threadIdx.x;
  os[t]=o[r*D_+t]; os[t+256]=o[r*D_+t+256]; __syncthreads();
  float a=0,b=0; for(int k=0;k<D_;k++){ a+=os[k]*bf2f(wo[k*D_+t]); b+=os[k]*bf2f(wo[k*D_+t+256]); }
  a+=bf2f(x[r*D_+t]); b+=bf2f(x[r*D_+t+256]);
  float s = a+b; for(int o=32;o;o>>=1) s += __shfl_xor(s,o);
  if((t&63)==0) red[t>>6]=s; __syncthreads();
  float m = (red[0]+red[1]+red[2]+red[3])/D_; __syncthreads();
  float v = (a-m)*(a-m)+(b-m)*(b-m); for(int o=32;o;o>>=1) v += __shfl_xor(v,o);
  if((t&63)==0) red[t>>6]=v; __syncthreads();
  float rs = rsqrtf((red[0]+red[1]+red[2]+red[3])/D_+1e-5f); __syncthreads();
  float na=(a-m)*rs, nb=(b-m)*rs;
  hn[r*D_+t]=na; hn[r*D_+t+256]=nb; hh[r*D_+t]=a; hh[r*D_+t+256]=b;
  out[r*D_+t]=f2bf(a+na); out[r*D_+t+256]=f2bf(b+nb);
  float d = na*bf2f(wr[t])+nb*bf2f(wr[t+256]); for(int o=32;o;o>>=1) d += __shfl_xor(d,o);
  if((t&63)==0) red[t>>6]=d; __syncthreads();
  if(t==0) rr[r]=red[0]+red[1]+red[2]+red[3];
}
__device__ int argmax_r(const float* rr, float* bv, int* bi){
  int t=threadIdx.x; float m=-INFINITY; int mi=0;
  for(int i=t;i<S_;i+=256){ float v=rr[i]; if(v>m){m=v;mi=i;} }
  bv[t]=m; bi[t]=mi; __syncthreads();
  for(int o=128;o;o>>=1){ if(t<o){ if(bv[t+o]>bv[t] || (bv[t+o]==bv[t] && bi[t+o]<bi[t])){bv[t]=bv[t+o];bi[t]=bi[t+o];} } __syncthreads(); }
  return bi[0];
}
__global__ void k_gu(const float* rr, const float* hn, const u16* wg, const u16* wu, float* act){
  __shared__ float bv[256]; __shared__ int bi[256];
  int idx = argmax_r(rr,bv,bi);
  int f = blockIdx.x*256+threadIdx.x; float g=0,u=0;
  for(int k=0;k<D_;k++){ float xv=hn[idx*D_+k]; g+=xv*bf2f(wg[k*F_+f]); u+=xv*bf2f(wu[k*F_+f]); }
  act[f]=g/(1.f+__expf(-g))*u;
}
__global__ void k_down(const float* rr, const float* hn, const float* hh, const float* act, const u16* wd, u16* out){
  __shared__ float bv[256]; __shared__ int bi[256];
  int idx = argmax_r(rr,bv,bi);
  int n = blockIdx.x*256+threadIdx.x; float y=0;
  for(int f=0;f<F_;f++) y+=act[f]*bf2f(wd[f*D_+n]);
  float sg = 1.f/(1.f+__expf(-rr[idx]));
  out[idx*D_+n]=f2bf(hh[idx*D_+n]+hn[idx*D_+n]+y*sg);
}
extern "C" void kernel_launch(void* const* d_in, const int* in_sizes, int n_in, void* d_out, int out_size, void* d_ws, size_t ws_size, hipStream_t stream){
  const u16* x=(const u16*)d_in[0]; const u16* wqkv=(const u16*)d_in[1]; const u16* wo=(const u16*)d_in[2];
  const u16* wr=(const u16*)d_in[4]; const u16* wg=(const u16*)d_in[5]; const u16* wu=(const u16*)d_in[6]; const u16* wd=(const u16*)d_in[7];
  float* ws=(float*)d_ws; float* qkv=ws; float* o=qkv+S_*1536; float* hn=o+S_*D_; float* hh=hn+S_*D_; float* rr=hh+S_*D_; float* act=rr+S_;
  k_ln_qkv<<<S_,256,0,stream>>>(x,wqkv,qkv);
  k_attn<<<S_*32/256,256,0,stream>>>(qkv,o);
  k_oproj<<<S_,256,0,stream>>>(o,wo,x,wr,hn,rr,(u16*)d_out,hh);
  k_gu<<<F_/256,256,0,stream>>>(rr,hn,wg,wu,act);
  k_down<<<D_/256,256,0,stream>>>(rr,hn,hh,act,wd,(u16*)d_out);
}
```

```cpp
#include <hip/hip_runtime.h>
#include <stdint.h>

typedef unsigned u32;
typedef unsigned short u16;
typedef unsigned long long u64;
typedef u32 u32x2 __attribute__((ext_vector_type(2)));
typedef u32 u32x4 __attribute__((ext_vector_type(4)));
typedef float f32x2 __attribute__((ext_vector_type(2)));
typedef float f32x4 __attribute__((ext_vector_type(4)));
typedef float f32x16 __attribute__((ext_vector_type(16)));
typedef __bf16 bf16x2 __attribute__((ext_vector_type(2)));
typedef __bf16 bf16x8 __attribute__((ext_vector_type(8)));
typedef short s16x4 __attribute__((ext_vector_type(4)));
typedef __amdgpu_buffer_rsrc_t rsrc_t;
#define LAS __attribute__((address_space(3)))
#define DI __device__ __forceinline__
#define UNROLL _Pragma("unroll")

constexpr u32 WS_E = 0;
constexpr u32 WS_XS = 4096;
constexpr u32 WS_ST = WS_XS + 32768;
constexpr u32 WS_CA = WS_ST + 131072;
constexpr u32 WS_AC = WS_CA + 98304;
constexpr u32 WS_KV = 1u << 20;
constexpr u32 WS_OF = 6u << 20;
constexpr u32 WS_STAMP = 12u << 20;
constexpr u32 WS_BYTES = 16u << 20;
constexpr u32 L_RING = 0, SLOT = 24576;
constexpr u32 L_WQ = 73728;
constexpr u32 L_KV = 106496;
constexpr u32 L_MISC = 155648;
constexpr u32 L_ONES = L_MISC;
constexpr u32 L_CS = L_MISC + 256;
constexpr u32 L_CSQ = L_MISC + 512;
constexpr u32 L_BEST = L_MISC + 1280;
constexpr u32 L_MR = L_MISC + 1536;
constexpr u32 L_TAB = L_MISC + 2048;
constexpr u32 L_QS = L_MISC + 4096;
constexpr u32 L_WO = 0;
constexpr u32 L_HT = 0;
constexpr u32 L_FF = 32768;
constexpr u32 SPIN_CAP = 1u << 16;

#define DLY_KV 0
#define DLY_O 10
#define DLY_ST 4
#define DLY_CA 8
#define DLY_AC 12

DI rsrc_t mk(const void* p, u32 bytes) { return __builtin_amdgcn_make_buffer_rsrc((void*)p, 0, bytes, 0x00020000); }
DI u32x4 mks(const void* p, u32 bytes) { u64 a = (u64)p; return u32x4{(u32)a, (u32)(a >> 32) & 0xffffu, bytes, 0x00020000u}; }
DI u32x4 ld16(rsrc_t r, u32 v, u32 s) { return __builtin_amdgcn_raw_buffer_load_b128(r, v, s, 0); }
DI u32x2 ld8(rsrc_t r, u32 v, u32 s) { return __builtin_amdgcn_raw_buffer_load_b64(r, v, s, 0); }
DI u32x4 ld16d(rsrc_t r, u32 v, u32 s) { return __builtin_amdgcn_raw_buffer_load_b128(r, v, s, 16); }
DI u32x2 ld8d(rsrc_t r, u32 v, u32 s) { return __builtin_amdgcn_raw_buffer_load_b64(r, v, s, 16); }
DI u32 ld4d(rsrc_t r, u32 v, u32 s) { return __builtin_amdgcn_raw_buffer_load_b32(r, v, s, 16); }
DI void st16d(u32x4 d, rsrc_t r, u32 v, u32 s) { __builtin_amdgcn_raw_buffer_store_b128(d, r, v + s, 0, 16); }
DI void st8d(u32x2 d, rsrc_t r, u32 v, u32 s) { __builtin_amdgcn_raw_buffer_store_b64(d, r, v, s, 16); }
DI void st4d(u32 d, rsrc_t r, u32 v, u32 s) { __builtin_amdgcn_raw_buffer_store_b32(d, r, v, s, 16); }
DI void dma16(u32x4 rs, u32 voff, u32 soff, u32 lds) {
  asm volatile("s_mov_b32 m0, %3\n\ts_nop 0\n\tbuffer_load_dwordx4 %0, %1, %2 offen lds" ::"v"(voff), "s"(rs), "s"(soff), "s"(lds) : "memory");
}
#define WAITVM(n) asm volatile("s_waitcnt vmcnt(" #n ")" ::: "memory")
DI void bar() { asm volatile("s_waitcnt lgkmcnt(0)\n\ts_barrier" ::: "memory"); }
DI void cfence() { asm volatile("" ::: "memory"); }
DI float asf(u32 x) { return __builtin_bit_cast(float, x); }
DI u32 asu(float x) { return __builtin_bit_cast(u32, x); }
DI float blo(u32 x) { return asf(x << 16); }
DI float bhi(u32 x) { return asf(x & 0xffff0000u); }
DI u32 pk(float a, float b) { return __builtin_bit_cast(u32, __builtin_convertvector(f32x2{a, b}, bf16x2)); }
DI u32 sum4(u32x4 a) { return a.x + a.y + a.z + a.w; }
DI f32x16 mfma32(u32x4 a, u32x4 b, f32x16 c) {
  return __builtin_amdgcn_mfma_f32_32x32x16_bf16(__builtin_bit_cast(bf16x8, a), __builtin_bit_cast(bf16x8, b), c, 0, 0, 0);
}
DI f32x4 mfma16(u32x4 a, u32x4 b, f32x4 c) {
  return __builtin_amdgcn_mfma_f32_16x16x32_bf16(__builtin_bit_cast(bf16x8, a), __builtin_bit_cast(bf16x8, b), c, 0, 0, 0);
}
template <int C> DI u32 dppu(u32 x) { return __builtin_amdgcn_update_dpp(0u, x, C, 0xf, 0xf, true); }
template <int C> DI float dppf(float x) { return asf(dppu<C>(asu(x))); }
DI float add_x16(float x) { auto r = __builtin_amdgcn_permlane16_swap(asu(x), asu(x), false, false); return asf(r[0]) + asf(r[1]); }
DI float add_x32(float x) { auto r = __builtin_amdgcn_permlane32_swap(asu(x), asu(x), false, false); return asf(r[0]) + asf(r[1]); }
DI float max_x32(float x) { auto r = __builtin_amdgcn_permlane32_swap(asu(x), asu(x), false, false); return fmaxf(asf(r[0]), asf(r[1])); }
DI float allsum32(float x) {
  x += dppf<0xB1>(x); x += dppf<0x4E>(x); x += dppf<0x141>(x); x += dppf<0x140>(x);
  return add_x16(x);
}
DI u32 umax(u32 a, u32 b) { return a > b ? a : b; }
DI u32 allmax32(u32 x) {
  x = umax(x, dppu<0xB1>(x)); x = umax(x, dppu<0x4E>(x)); x = umax(x, dppu<0x141>(x)); x = umax(x, dppu<0x140>(x));
  auto r = __builtin_amdgcn_permlane16_swap(x, x, false, false);
  return umax(r[0], r[1]);
}
DI bool allok(bool ok) { return __builtin_amdgcn_ballot_w64(!ok) == 0; }
DI int crow(int r, int h) { return (r & 3) + 8 * (r >> 2) + 4 * h; }

#define STAMP(n)
#define STAMPV(n, v)

__global__ void __launch_bounds__(512, 2)
fused_block(const u16* __restrict__ x, const u16* __restrict__ wqkv, const u16* __restrict__ wo, const u16* __restrict__ wr,
            const u16* __restrict__ wg, const u16* __restrict__ wu, const u16* __restrict__ wd, u16* __restrict__ out, char* ws) {
  __shared__ __attribute__((aligned(1024))) char lds_[163840];
  LAS char* const lds = (LAS char*)lds_;
#define LD128(a) (*(LAS u32x4*)(lds + (a)))
#define LDF4(a) (*(LAS f32x4*)(lds + (a)))
#define LDF2(a) (*(LAS f32x2*)(lds + (a)))
#define LDF(a) (*(LAS float*)(lds + (a)))
#define LDU(a) (*(LAS u32*)(lds + (a)))
  auto trfrag = [&](u32 a0, u32 a1) -> u32x4 {
    u32x2 p = __builtin_bit_cast(u32x2, __builtin_amdgcn_ds_read_tr16_b64_v4i16((LAS s16x4*)(lds + a0)));
    u32x2 q = __builtin_bit_cast(u32x2, __builtin_amdgcn_ds_read_tr16_b64_v4i16((LAS s16x4*)(lds + a1)));
    return u32x4{p.x, p.y, q.x, q.y};
  };
  STAMP(0);
  const u32 tid = threadIdx.x, l = tid & 63, h = l >> 5, l31 = l & 31;
  const u32 w = __builtin_amdgcn_readfirstlane(tid >> 6);
  const u32 b = blockIdx.x, g = b & 7, i = b >> 3;
  const u32 tb = (g >> 1) * 4 + (i & 3), hp = (g & 1) * 8 + (i >> 2);
  const u32 pn = i >> 2, tq = g * 8 + pn, cg = i & 3;
  const u32 rot = (pn + 2 * (i & 3)) & 7;
  const u32 epoch = *(const __attribute__((address_space(4))) u32*)(ws + WS_E);
  const u32 tag = (epoch + 1u) * 0x9E3779B1u + 0x7F4A7C15u;
  const rsrc_t rws = mk(ws, WS_BYTES), rx = mk(x, 2048 * 1024), rout = mk(out, 2048 * 1024);
  const u32 trl = ((l & 15) >> 2) * 64 + ((l >> 4) & 1) * 32 + (l & 3) * 8;
  const bool mw = w < 4;
  const u32 wj = w & 3;

  u32x4 qf;
  u32x4 wof[32];

  if (!mw) {
    const u32x4 sx = mks(x, 2048 * 1024), sw = mks(wqkv, 512 * 3072), so = mks(wo, 512 * 1024);
    const u32 vx0 = (l >> 3) * 1024 + (((l & 7) ^ (l >> 4)) << 4), vx1 = vx0 ^ 64;
    const u32 vw = (l >> 2) * 3072 + (l & 3) * 16, vo = (l >> 2) * 1024 + (l & 3) * 16;
    const u32 xrow = (tb * 128 + wj * 32) * 1024, wrow = wj * 16 * 3072 + hp * 64;
    auto chunk = [&](u32 kc, u32 slot) {
      const u32 kk = (kc + rot) & 7, sb = L_RING + slot * SLOT;
      const u32 sxo = xrow + kk * 128, swo = wrow + kk * (64 * 3072);
      dma16(sx, vx0, sxo, sb + wj * 4096);
      dma16(sx, vx1, sxo + 8192, sb + wj * 4096 + 1024);
      dma16(sx, vx0, sxo + 16384, sb + wj * 4096 + 2048);
      dma16(sx, vx1, sxo + 24576, sb + wj * 4096 + 3072);
      dma16(sw, vw, swo + 1024, sb + 16384 + wj * 1024);
      dma16(sw, vw, swo + 2048, sb + 16384 + 4096 + wj * 1024);
    };
    chunk(0, 0);
    chunk(1, 1);
    const u32 one2 = ((((l & 15) >> 2) ^ (l >> 4)) & 1) ? 0u : 0x3f803f80u;
    const u32x4 sel = {one2, one2, one2, one2};
    if (l < 32) LD128(L_ONES + (l & 15) * 16 + (l >> 4) * 1024) = u32x4{0x3f803f80u, 0x3f803f80u, 0x3f803f80u, 0x3f803f80u};
    f32x4 cs = {0, 0, 0, 0};
    u32 slot = 0;
    for (u32 kc = 0; kc < 8; ++kc) {
      if (kc < 7) WAITVM(6); else WAITVM(8);
      bar();
      u32x4 f[4];
      if (wj < 2) {
        const u32 a = L_RING + slot * SLOT + 16384 + wj * 4096 + h * 512 + trl;
        UNROLL for (int s = 0; s < 4; ++s) f[s] = trfrag(a + s * 1024, a + s * 1024 + 256);
      }
      const u32 nslot = slot == 0 ? 2 : slot - 1;
      if (kc < 6) chunk(kc + 2, nslot);
      else if (kc == 6) {
        UNROLL for (int c = 0; c < 8; ++c) dma16(sw, vw, wrow + ((c + rot) & 7) * (64 * 3072), L_WQ + (c * 4 + wj) * 1024);
      }
      if (wj < 2) { UNROLL for (int s = 0; s < 4; ++s) cs = mfma16(sel, f[s], cs); }
      slot = slot == 2 ? 0 : slot + 1;
    }
    if (wj < 2 && l < 32) LDF(L_CS + wj * 128 + l * 4) = cs[0];
    bar();
    const u32 ocol = cg * 256 + wj * 64;
    UNROLL for (int a = 0; a < 8; ++a) dma16(so, vo, ((a + 4 * pn) & 31) * 16384 + ocol, L_KV + (a * 4 + wj) * 1024);
    WAITVM(8);
    f32x4 cq = {0, 0, 0, 0};
    UNROLL for (int c = 0; c < 8; ++c) {
      const u32 a = L_WQ + (c * 4 + wj) * 1024 + h * 512 + trl;
      cq = mfma16(sel, trfrag(a, a + 256), cq);
    }
    if (l < 32) LDF(L_CSQ + l * 16 + wj * 4) = cq[0];
    bar();
    for (u32 a = 8; a < 26; ++a) dma16(so, vo, ((a + 4 * pn) & 31) * 16384 + ocol, L_WO + ((a - 8) * 4 + wj) * 1024);
    WAITVM(18);
    bar();
    for (u32 a = 26; a < 32; ++a) dma16(so, vo, ((a + 4 * pn) & 31) * 16384 + ocol, L_WO + ((a - 8) * 4 + wj) * 1024);
    qf = LD128(L_QS + (3 - wj) * 1024 + l * 16);
  } else {
    {
      const u32 row = tb * 128 + hp * 8 + 2 * wj + h;
      const u32x4 a = ld16(rx, row * 1024 + l31 * 16, 0), c = ld16(rx, row * 1024 + l31 * 16 + 512, 0);
      float v[16];
      UNROLL for (int e = 0; e < 4; ++e) { v[2 * e] = blo(a[e]); v[2 * e + 1] = bhi(a[e]); v[8 + 2 * e] = blo(c[e]); v[9 + 2 * e] = bhi(c[e]); }
      float s = 0;
      UNROLL for (int e = 0; e < 16; ++e) s += v[e];
      const float mean = allsum32(s) * (1.f / 512);
      float q = 0;
      UNROLL for (int e = 0; e < 16; ++e) { const float d = v[e] - mean; q += d * d; }
      const float rstd = __builtin_amdgcn_rsqf(allsum32(q) * (1.f / 512) + 1e-5f);
      const u32 d0 = asu(mean * rstd), d1 = asu(rstd);
      if (l31 == 0) st16d(u32x4{d0, d1, 0u, tag + d0 + d1}, rws, WS_XS + row * 16, 0);
    }
    STAMP(1);
    f32x16 ak, av;
    UNROLL for (int r = 0; r < 16; ++r) { ak[r] = 0; av[r] = 0; }
    const u32 xa = L_RING + (wj * 32 + l31) * 128 + ((h ^ ((l31 >> 1) & 7)) << 4);
    const u32 wa = L_RING + 16384 + h * 512 + trl;
    const u32 tab = L_TAB + wj * 384;
    u32x4 xs, xfr[32];
    UNROLL for (int kc = 0; kc < 8; ++kc) {
      bar();
      if (kc == 0) STAMP(2);
      const u32 sb = (kc % 3) * SLOT;
      UNROLL for (int s = 0; s < 4; ++s) {
        const u32x4 xf = LD128((xa ^ (s << 5)) + sb);
        const u32x4 kf = trfrag(wa + sb + s * 1024, wa + sb + s * 1024 + 256);
        const u32x4 vf = trfrag(wa + sb + 4096 + s * 1024, wa + sb + 4096 + s * 1024 + 256);
        xfr[kc * 4 + s] = xf;
        ak = mfma32(kf, xf, ak);
        av = mfma32(xf, vf, av);
      }
      if (kc == 4) xs = ld16d(rws, WS_XS + (tb * 128 + wj * 32 + l31) * 16, 0);
      if (kc == 6) {
        u32 spin = 0;
        while (!allok(xs.w == tag + xs.x + xs.y + xs.z) && ++spin < SPIN_CAP) {
          cfence();
          xs = ld16d(rws, WS_XS + (tb * 128 + wj * 32 + l31) * 16, 0);
        }
        STAMPV(25, spin);
        LDU(tab + h * 128 + l31 * 4) = h ? xs.y : xs.x;
      }
    }
    bar();
    STAMP(3);
    const float mr = asf(xs.x), rstd = asf(xs.y);
    u32x4 kv[4];
    {
      const float csv = LDF(L_CS + 128 + l31 * 4);
      UNROLL for (int q = 0; q < 4; ++q) {
        const f32x4 c = LDF4(L_CS + q * 32 + h * 16);
        float t[4];
        UNROLL for (int e = 0; e < 4; ++e) t[e] = ak[4 * q + e] * rstd - mr * c[e];
        kv[q >> 1][(q & 1) * 2] = pk(t[0], t[1]);
        kv[q >> 1][(q & 1) * 2 + 1] = pk(t[2], t[3]);
      }
      UNROLL for (int q = 0; q < 4; ++q) {
        const f32x4 m4 = LDF4(tab + q * 32 + h * 16), r4 = LDF4(tab + 128 + q * 32 + h * 16);
        float t[4];
        UNROLL for (int e = 0; e < 4; ++e) t[e] = av[4 * q + e] * r4[e] - m4[e] * csv;
        kv[2 + (q >> 1)][(q & 1) * 2] = pk(t[0], t[1]);
        kv[2 + (q >> 1)][(q & 1) * 2 + 1] = pk(t[2], t[3]);
      }
    }
    const u32 rec = WS_KV + (hp * 64 + tb * 4 + wj) * 4352;
    if (tb < 15) {
      UNROLL for (int q = 0; q < 4; ++q) st16d(kv[q], rws, l * 16 + q * 1024, rec);
      st4d(tag + sum4(kv[0]) + sum4(kv[1]) + sum4(kv[2]) + sum4(kv[3]), rws, l * 4, rec + 4096);
    }
    UNROLL for (int q = 0; q < 4; ++q) LD128(L_KV + (8 + wj) * 4096 + q * 1024 + l * 16) = kv[q];
    STAMP(4);
    bar();
    {
      const f32x4 p = LDF4(L_CSQ + l31 * 16);
      LDF(tab + 256 + l31 * 4) = (p[0] + p[1]) + (p[2] + p[3]);
    }
    f32x16 aq;
    UNROLL for (int r = 0; r < 16; ++r) aq[r] = 0;
    const u32 qa = L_WQ + h * 512 + trl;
    UNROLL for (int a = 0; a < 32; ++a) aq = mfma32(trfrag(qa + a * 1024, qa + a * 1024 + 256), xfr[a], aq);
    const float sc = 0.25f * 1.44269504088896341f, rq = rstd * sc, mq = mr * sc;
    u32x4 q2[2];
    UNROLL for (int q = 0; q < 4; ++q) {
      const f32x4 c = LDF4(tab + 256 + q * 32 + h * 16);
      float t[4];
      UNROLL for (int e = 0; e < 4; ++e) t[e] = aq[4 * q + e] * rq - mq * c[e];
      q2[q >> 1][(q & 1) * 2] = pk(t[0], t[1]);
      q2[q >> 1][(q & 1) * 2 + 1] = pk(t[2], t[3]);
    }
    LD128(L_QS + wj * 1024 + l * 16) = q2[1];
    qf = q2[0];
    bar();
    const u32 oa = L_KV + wj * 1024 + h * 256 + trl;
    UNROLL for (int a = 0; a < 8; ++a) wof[a] = trfrag(oa + a * 4096, oa + a * 4096 + 512);
  }
  STAMP(5);

  const u32 tt = mw ? wj : 3 - wj, hd = mw ? 0 : 1;
  const u32 ka0 = L_KV + hd * 1024 + l * 16;
  const bool vd = (l & 16) == 0;
  const u32 va0 = vd ? L_KV + 2048 + ((l & 15) + 16 * hd + 32 * h) * 16 : L_ONES + (l & 15) * 16;
  const u32 vstep = vd ? 4096 : 0;
  f32x16 o, cn;
  const float NINF = -__builtin_inff();
  auto pv = [&](const f32x16& s, u32 j) {
    const u32 va = va0 + j * vstep;
    const u32x4 v0 = LD128(va), v1 = LD128(va + 1024);
    u32x4 p0, p1;
    UNROLL for (int e = 0; e < 4; ++e) p0[e] = pk(__builtin_amdgcn_exp2f(s[2 * e]), __builtin_amdgcn_exp2f(s[2 * e + 1]));
    o = mfma32(v0, p0, o);
    UNROLL for (int e = 0; e < 4; ++e) p1[e] = pk(__builtin_amdgcn_exp2f(s[8 + 2 * e]), __builtin_amdgcn_exp2f(s[9 + 2 * e]));
    o = mfma32(v1, p1, o);
  };
  float m;
  {
    f32x16 c;
    UNROLL for (int r = 0; r < 16; ++r) { c[r] = (u32)crow(r, h) <= l31 ? 0.f : NINF; o[r] = 0; }
    f32x16 s = mfma32(LD128(ka0 + (8 + tt) * 4096), qf, c);
    m = s[0];
    UNROLL for (int r = 1; r < 16; ++r) m = fmaxf(m, s[r]);
    m = max_x32(m);
    UNROLL for (int r = 0; r < 16; ++r) { s[r] -= m; cn[r] = -m; }
    pv(s, 8 + tt);
  }
  for (u32 j = 8; j < 8 + tt; ++j) pv(mfma32(LD128(ka0 + j * 4096), qf, cn), j);
  STAMP(6);
  const int jmin = 8 - 4 * (int)tb;
  bar();
  if ((int)w >= jmin) {
    if (DLY_KV) __builtin_amdgcn_s_sleep(DLY_KV);
    const u32 rec = WS_KV + (hp * 64 + tb * 4 - 8 + w) * 4352;
    u32x4 f[4];
    u32 spin = 0;
    for (;;) {
      UNROLL for (int q = 0; q < 4; ++q) f[q] = ld16d(rws, l * 16 + q * 1024, rec);
      const u32 ck = ld4d(rws, l * 4, rec + 4096);
      if (allok(ck == tag + sum4(f[0]) + sum4(f[1]) + sum4(f[2]) + sum4(f[3])) || ++spin >= SPIN_CAP) break;
      cfence();
    }
    STAMPV(26, spin);
    UNROLL for (int q = 0; q < 4; ++q) LD128(L_KV + w * 4096 + q * 1024 + l * 16) = f[q];
  }
  STAMP(7);
  bar();
  STAMP(8);
  u32x4 fw[7];
  u32x2 xres[4], wrv[4];
  const rsrc_t rwr = mk(wr, 1024);
  const u32x4 wrall = ld16(rwr, l * 16, 0);
  if (mw) {
    UNROLL for (int q = 0; q < 4; ++q) {
      xres[q] = ld8(rx, (tq * 32 + l31) * 1024 + h * 8 + q * 16, cg * 256 + wj * 64);
      wrv[q] = ld8(rwr, h * 8 + q * 16, cg * 256 + wj * 64);
    }
  }
  if (g == 0) {
    const rsrc_t rd = mk(wd, 1792 * 1024);
    UNROLL for (int q = 0; q < 7; ++q) fw[q] = ld16(rd, (l >> 1) * 1024 + (l & 1) * 16, (w * 224 + q * 32) * 1024 + i * 32);
  } else if (!mw) {
    const u16* p = ((l & 1) ? wu : wg) + (size_t)(wj * 128 + (l >> 1)) * 1792 + ((g - 1) * 32 + i) * 8;
    UNROLL for (int q = 0; q < 4; ++q) fw[q] = *(const __attribute__((address_space(1))) u32x4*)(p + q * 32 * 1792);
  }
  cfence();
  if ((int)tt >= jmin) {
    f32x16 c;
    UNROLL for (int r = 0; r < 16; ++r) c[r] = (u32)crow(r, h) > l31 ? -m : NINF;
    pv(mfma32(LD128(ka0 + tt * 4096), qf, c), tt);
  }
  for (int j = (int)tt + 1 > jmin ? (int)tt + 1 : jmin; j < 8; ++j) pv(mfma32(LD128(ka0 + j * 4096), qf, cn), j);
  if (!allok(o[8] < 1.2676506e30f)) {
    const int j0 = (int)tt > jmin ? (int)tt : jmin;
    auto masked = [&](int j, float c0) {
      f32x16 c;
      UNROLL for (int r = 0; r < 16; ++r) {
        const int d = (j - (int)tt) * 32 + crow(r, h) - (int)l31;
        c[r] = (d > 0 && d <= 256) ? c0 : NINF;
      }
      return mfma32(LD128(ka0 + j * 4096), qf, c);
    };
    float mt = NINF;
    for (int j = j0; j <= 8 + (int)tt; ++j) {
      const f32x16 s = masked(j, 0.f);
      UNROLL for (int r = 0; r < 16; ++r) mt = fmaxf(mt, s[r]);
    }
    mt = max_x32(mt);
    UNROLL for (int r = 0; r < 16; ++r) o[r] = 0;
    for (int j = j0; j <= 8 + (int)tt; ++j) pv(masked(j, -mt), j);
  }
  {
    const float inv = __builtin_amdgcn_rcpf(o[8]);
    u32x4 of;
    UNROLL for (int e = 0; e < 4; ++e) of[e] = pk(o[2 * e] * inv, o[2 * e + 1] * inv);
    const u32 rec = WS_OF + ((tb * 4 + tt) * 32 + hp * 2 + hd) * 1280;
    st16d(of, rws, l * 16, rec);
    st4d(tag + sum4(of), rws, l * 4, rec + 1024);
  }
  STAMP(9);
  if (!mw) WAITVM(2);
  bar();
  STAMP(10);

  f32x16 hh;
  float wrf[16];
  float swr;
  {
    float s = 0;
    UNROLL for (int e = 0; e < 4; ++e) s += blo(wrall[e]) + bhi(wrall[e]);
    swr = add_x32(allsum32(s));
  }
  if (mw) {
    const u32 oa = L_WO + wj * 1024 + h * 256 + trl;
    UNROLL for (int a = 8; a < 32; ++a) wof[a] = trfrag(oa + (a - 8) * 4096, oa + (a - 8) * 4096 + 512);
    UNROLL for (int q = 0; q < 4; ++q) {
      hh[4 * q] = blo(xres[q].x); hh[4 * q + 1] = bhi(xres[q].x); hh[4 * q + 2] = blo(xres[q].y); hh[4 * q + 3] = bhi(xres[q].y);
      wrf[4 * q] = blo(wrv[q].x); wrf[4 * q + 1] = bhi(wrv[q].x); wrf[4 * q + 2] = blo(wrv[q].y); wrf[4 * q + 3] = bhi(wrv[q].y);
    }
  }
  {
    if (DLY_O) __builtin_amdgcn_s_sleep(DLY_O);
    const u32 rec = WS_OF + (tq * 32 + ((w + pn) & 7) * 4) * 1280;
    u32x4 f[4];
    u32 spin = 0;
    for (;;) {
      u32 ck[4];
      UNROLL for (int q = 0; q < 4; ++q) { f[q] = ld16d(rws, l * 16, rec + q * 1280); ck[q] = ld4d(rws, l * 4 + 1024, rec + q * 1280); }
      bool ok = true;
      UNROLL for (int q = 0; q < 4; ++q) ok &= ck[q] == tag + sum4(f[q]);
      if (allok(ok) || ++spin >= SPIN_CAP) break;
      cfence();
    }
    STAMPV(27, spin);
    UNROLL for (int q = 0; q < 4; ++q) LD128(L_KV + (w * 4 + q) * 1024 + l * 16) = f[q];
  }
  STAMP(11);
  bar();
  STAMP(12);
  if (mw) {
    UNROLL for (int a = 0; a < 32; ++a) hh = mfma32(wof[a], LD128(L_KV + a * 1024 + l * 16), hh);
    float s1 = 0, s2 = 0, s3 = 0;
    UNROLL for (int r = 0; r < 16; ++r) { s1 += hh[r]; s2 += hh[r] * hh[r]; s3 += hh[r] * wrf[r]; }
    LDF4(L_QS + wj * 1024 + l * 16) = f32x4{s1, s2, s3, 0.f};
  }
  bar();
  STAMP(13);
  if (w == 4) {
    f32x4 p = LDF4(L_QS + l * 16);
    UNROLL for (int q = 1; q < 4; ++q) p += LDF4(L_QS + q * 1024 + l * 16);
    const u32 d0 = asu(add_x32(p[0])), d1 = asu(add_x32(p[1])), d2 = asu(add_x32(p[2]));
    if (l < 32) st16d(u32x4{d0, d1, d2, tag + d0 + d1 + d2}, rws, l * 16, WS_ST + (tq * 4 + cg) * 512);
  }
  if (mw) {
    UNROLL for (int q = 0; q < 4; ++q)
      LDF4(L_HT + l31 * 528 + wj * 128 + q * 32 + h * 16) = f32x4{hh[4 * q], hh[4 * q + 1], hh[4 * q + 2], hh[4 * q + 3]};
  }
  {
    const u32 fb = L_FF + w * 7168;
    const u32 tl = ((l & 15) >> 2) * 32 + (l & 3) * 8;
    if (g == 0) {
      UNROLL for (int q = 0; q < 7; ++q) LD128(fb + q * 1024 + l * 16) = fw[q];
      cfence();
      UNROLL for (int s = 0; s < 7; ++s) { const u32 a = fb + ((l >> 4) * 7 + s) * 256 + tl; fw[s] = trfrag(a, a + 128); }
    } else if (!mw) {
      UNROLL for (int q = 0; q < 4; ++q) LD128(fb + q * 1024 + l * 16) = fw[q];
      cfence();
      UNROLL for (int p = 0; p < 4; ++p) { const u32 a = fb + ((l >> 4) * 4 + p) * 256 + tl; fw[p] = trfrag(a, a + 128); }
    }
  }
  {
    if (DLY_ST) __builtin_amdgcn_s_sleep(DLY_ST);
    const u32 rec = WS_ST + (g * 8 + w) * 2048;
    u32x4 a, c;
    u32 spin = 0;
    for (;;) {
      a = ld16d(rws, l * 16, rec);
      c = ld16d(rws, l * 16 + 1024, rec);
      if (allok(a.w == tag + a.x + a.y + a.z && c.w == tag + c.x + c.y + c.z) || ++spin >= SPIN_CAP) break;
      cfence();
    }
    STAMPV(28, spin);
    const float mean = add_x32(asf(a.x) + asf(c.x)) * (1.f / 512);
    const float var = fmaxf(add_x32(asf(a.y) + asf(c.y)) * (1.f / 512) - mean * mean, 0.f);
    const float rstd = __builtin_amdgcn_rsqf(var + 1e-5f);
    const float r = (add_x32(asf(a.z) + asf(c.z)) - mean * swr) * rstd;
    u32 key = asu(r);
    key ^= (key >> 31) ? 0xffffffffu : 0x80000000u;
    key = (key & ~2047u) | (2047u - ((g * 8 + w) * 32 + l31));
    key = allmax32(key);
    if (l == 0) LDU(L_BEST + w * 4) = key;
    if (w == pn && l < 32) LDF2(L_MR + l * 8) = f32x2{mean, rstd};
  }
  STAMP(14);
  bar();
  u32 gkey;
  {
    const u32x4 a = LD128(L_BEST), c = LD128(L_BEST + 16);
    gkey = umax(umax(umax(a.x, a.y), umax(a.z, a.w)), umax(umax(c.x, c.y), umax(c.z, c.w)));
    gkey = __builtin_amdgcn_readfirstlane(gkey);
  }
  const u32 gtok = 2047u - (gkey & 2047u);
  const bool mine = (gtok >> 5) == tq;
  if (mine && w < 2) {
    const u32 row = gtok & 31, rep = w * 4 + (l >> 4), ch = l & 15;
    const f32x4 a = LDF4(L_HT + row * 528 + ch * 32), c = LDF4(L_HT + row * 528 + ch * 32 + 16);
    const f32x2 mr = LDF2(L_MR + row * 8);
    const float k1 = rep == 0 ? mr.y + 1.f : mr.y, k0 = -mr.x * mr.y;
    const u32x4 d = {pk(a[0] * k1 + k0, a[1] * k1 + k0), pk(a[2] * k1 + k0, a[3] * k1 + k0), pk(c[0] * k1 + k0, c[1] * k1 + k0), pk(c[2] * k1 + k0, c[3] * k1 + k0)};
    const u32 rec = WS_CA + (g * 4 + cg) * 384;
    st16d(d, rws, rep * 12288 + ch * 16, rec);
    st8d(u32x2{gkey, tag + gkey + sum4(d)}, rws, rep * 12288 + 256 + ch * 8, rec);
  }
  STAMP(15);
  u32x4 res;
  const u32 orow = tid >> 4;
  const u32 ooff = (tq * 32 + orow) * 1024 + cg * 256 + (tid & 15) * 16;
  const bool held = mine && orow == (gtok & 31);
  {
    const f32x4 a = LDF4(L_HT + orow * 528 + (tid & 15) * 32), c = LDF4(L_HT + orow * 528 + (tid & 15) * 32 + 16);
    const f32x2 mr = LDF2(L_MR + orow * 8);
    const float k1 = mr.y + 1.f, k0 = -mr.x * mr.y;
    res = u32x4{pk(a[0] * k1 + k0, a[1] * k1 + k0), pk(a[2] * k1 + k0, a[3] * k1 + k0), pk(c[0] * k1 + k0, c[1] * k1 + k0), pk(c[2] * k1 + k0, c[3] * k1 + k0)};
    if (!held) st16d(res, rout, ooff, 0);
  }
  STAMP(16);

  u32 wkey = 0;
  float base = 0;
  if (!mw || g == 0) {
    if (DLY_CA) __builtin_amdgcn_s_sleep(DLY_CA);
    const u32 sl = g == 0 ? i >> 3 : wj;
    const u32 ch = (l >> 4) * 4 + (l & 3), cq = (l >> 2) & 3;
    const u32 rec = WS_CA + g * 12288 + sl * 384;
    u32x4 a, c;
    u32x2 sa, sc;
    u32 spin = 0;
    for (;;) {
      a = ld16d(rws, cq * 1536 + ch * 16, rec);
      c = ld16d(rws, cq * 1536 + ch * 16, rec + 6144);
      sa = ld8d(rws, cq * 1536 + 256 + ch * 8, rec);
      sc = ld8d(rws, cq * 1536 + 256 + ch * 8, rec + 6144);
      if (allok(sa.y == tag + sa.x + sum4(a) && sc.y == tag + sc.x + sum4(c)) || ++spin >= SPIN_CAP) break;
      cfence();
    }
    STAMPV(29, spin);
    u32 k = umax(sa.x, sc.x);
    k = umax(k, dppu<0x124>(k));
    k = umax(k, dppu<0x128>(k));
    wkey = __builtin_amdgcn_readfirstlane(k);
    const u32 cand = (2047u - (wkey & 2047u)) >> 8;
    if (cand >= 4) a = c;
    STAMP(17);
    if (g != 0) {
      const f32x4 z = {0, 0, 0, 0};
      const f32x4 d0 = mfma16(a, fw[0], z), d1 = mfma16(a, fw[1], z), d2 = mfma16(a, fw[2], z), d3 = mfma16(a, fw[3], z);
      const float y = (d0[0] + d1[1]) + (d2[2] + d3[3]);
      if ((l >> 4) == (cand & 3)) LDF(L_QS + ((l >> 1) & 3) * 64 + wj * 16 + ((l >> 3) & 1) * 8 + (l & 1) * 4) = y;
      if (w == 4 && l == 0) LDU(L_BEST + 32) = wkey;
    } else {
      if (w == 0 && cq == (cand & 3) && (ch >> 1) == (i & 7)) LD128(L_QS + 2048 + (ch & 1) * 16) = a;
      if (b == 0 && tid == 0) st4d(epoch + 1u, rws, WS_E, 0);
      if (DLY_AC) __builtin_amdgcn_s_sleep(DLY_AC);
      const u32 ri = (l & 15) < 7 ? (l & 15) : 0;
      const u32 vo = (w * 28 + (l >> 4) * 7 + ri) * 128;
      u32x4 p, q;
      spin = 0;
      for (;;) {
        p = ld16d(rws, vo, WS_AC);
        q = ld16d(rws, vo + 16, WS_AC);
        if (allok(p.y == tag + p.x && p.w == tag + p.z && q.y == tag + q.x && q.w == tag + q.z) || ++spin >= SPIN_CAP) break;
        cfence();
      }
      STAMPV(30, spin);
      STAMP(18);
      const u32x4 af = {p.x, p.z, q.x, q.z};
      const f32x4 z = {0, 0, 0, 0};
      f32x4 d[7];
      UNROLL for (int s = 0; s < 7; ++s) d[s] = mfma16(af, fw[s], z);
      const float y0 = (d[0][0] + d[1][1]) + (d[2][2] + d[3][3]), y1 = (d[4][0] + d[5][1]) + d[6][2];
      if (l < 32) LDF(L_QS + (l & 15) * 64 + w * 8 + (l >> 4) * 4) = l < 16 ? y0 : y1;
    }
  }
  bar();
  if (g != 0) {
    wkey = __builtin_amdgcn_readfirstlane(LDU(L_BEST + 32));
    if (w == 0 && l < 4) {
      const f32x4 p = (LDF4(L_QS + l * 64) + LDF4(L_QS + l * 64 + 16)) + (LDF4(L_QS + l * 64 + 32) + LDF4(L_QS + l * 64 + 48));
      const float a0 = p[0] * __builtin_amdgcn_rcpf(1.f + __builtin_amdgcn_exp2f(-1.44269504088896341f * p[0])) * p[2];
      const float a1 = p[1] * __builtin_amdgcn_rcpf(1.f + __builtin_amdgcn_exp2f(-1.44269504088896341f * p[1])) * p[3];
      const u32 d = pk(a0, a1);
      st8d(u32x2{d, tag + d}, rws, l * 8, WS_AC + ((g - 1) * 32 + i) * 128);
    }
  } else if (w == 0) {
    const f32x4 p = LDF4(L_QS + (l & 15) * 64 + (l >> 4) * 16);
    const float y = add_x32(add_x16((p[0] + p[1]) + (p[2] + p[3])));
    base = asf((u32)(*(LAS u16*)(lds + L_QS + 2048 + (l & 15) * 2)) << 16);
    u32 rb = wkey & ~2047u;
    rb ^= (rb >> 31) ? 0x80000000u : 0xffffffffu;
    const float sg = __builtin_amdgcn_rcpf(1.f + __builtin_amdgcn_exp2f(-1.44269504088896341f * asf(rb)));
    const u32 tok = 2047u - (wkey & 2047u);
    if (l < 16) __builtin_amdgcn_raw_buffer_store_b16((u16)pk(base + sg * y, 0.f), rout, l * 2, tok * 1024 + i * 32, 16);
  }
  if (held && wkey != gkey) st16d(res, rout, ooff, 0);
  STAMP(19);
}

extern "C" void kernel_launch(void* const* d_in, const int* in_sizes, int n_in, void* d_out, int out_size, void* d_ws, size_t ws_size,
                              hipStream_t stream) {
  fused_block<<<256, 512, 0, stream>>>((const u16*)d_in[0], (const u16*)d_in[1], (const u16*)d_in[2], (const u16*)d_in[4],
                                       (const u16*)d_in[5], (const u16*)d_in[6], (const u16*)d_in[7], (u16*)d_out, (char*)d_ws);
}
```

```cpp
#include <hip/hip_runtime.h>
#include <stdint.h>
typedef unsigned u32;
typedef unsigned short u16;
constexpr u32 WS_STAMP = 12u << 20;

__global__ void __launch_bounds__(512, 2)
fused_block(const void* x, u32, u32, const void* wqkv, u32, u32, const void* wo, u32, u32, void* ws, u32, u32,
            const void* wg, u32, u32, const void* wu, u32, u32, const void* wd, u32, u32, void* out, u32, u32,
            const void* wr, u32, u32) {
  __shared__ __attribute__((aligned(1024))) char lds[163840];
#if defined(__HIP_DEVICE_COMPILE__)
  asm volatile(
".Lfb_start:\n"
"  s_load_dwordx16 s[8:23], s[0:1], 0x0\n"
"  v_readfirstlane_b32 s44, v0\n"
"  s_getpc_b64 s[4:5]\n"
"  s_lshr_b32 s47, s2, 3\n"
"  s_and_b32 s4, s4, 0xffffff00\n"
"  s_lshr_b32 s44, s44, 6\n"
"  s_and_b32 s5, s5, 0xffff\n"
"  s_mov_b32 s6, .Lfb_end-.Lfb_start\n"
"  s_lshl3_add_u32 s60, s47, s44\n"
"  s_mov_b32 s7, 0x20000\n"
"  s_lshl_b32 s60, s60, 7\n"
"  s_mov_b32 m0, 157440\n"
"  s_min_u32 s60, s60, .Lfb_end-.Lfb_start-4\n"
"  buffer_load_dword off, s[4:7], s60 lds\n"
"  v_and_b32 v255, 63, v0\n"
"  s_and_b32 s46, s2, 7\n"
"  s_and_b32 s45, s44, 3\n"
"  v_lshlrev_b32 v254, 4, v255\n"
"  s_lshr_b32 s60, s46, 1\n"
"  s_and_b32 s61, s47, 7\n"
"  s_lshl3_add_u32 s48, s60, s61\n"
"  s_and_b32 s60, s46, 1\n"
"  s_lshr_b32 s62, s47, 3\n"
"  s_lshl2_add_u32 s49, s60, s62\n"
"  s_cmp_lt_u32 s44, 4\n"
"  s_cbranch_scc1 .Lfb_M\n"
"  s_add_u32 s53, s62, s61\n"
"  s_mul_i32 s53, s53, 4\n"
"  s_lshr_b32 s61, s46, 1\n"
"  s_xor_b32 s61, s61, s46\n"
"  s_and_b32 s61, s61, 1\n"
"  s_mul_i32 s61, s61, 2\n"
"  s_add_u32 s53, s53, s61\n"
"  v_lshrrev_b32 v243, 3, v255\n"
"  v_and_b32 v244, 0x70, v254\n"
"  v_and_b32 v230, 0x30, v255\n"
"  v_xor_b32 v230, v230, v244\n"
"  v_lshl_or_b32 v230, v243, 10, v230\n"
"  v_and_b32 v234, 16, v255\n"
"  v_lshlrev_b32 v234, 2, v234\n"
"  v_xor_b32 v244, v234, v244\n"
"  v_xor_b32 v231, 64, v230\n"
"  v_mul_u32_u24 v234, 3072, v243\n"
"  v_add_u32 v231, 7168, v231\n"
"  v_or_b32 v234, v234, v244\n"
"  s_lshl2_add_u32 s86, s48, s45\n"
"  v_add_u32 v232, 1024, v234\n"
"  s_lshl_b32 s86, s86, 14\n"
"  v_add_u32 v233, 24576, v234\n"
"  s_mul_i32 s87, s45, 49152\n"
"  v_add_u32 v235, 23552, v234\n"
"  s_lshl_b32 s60, s49, 7\n"
"  s_add_u32 s87, s87, s60\n"
"  s_lshl_b32 s85, s45, 12\n"
"  s_lshl_b32 s84, s45, 11\n"
"  s_add_u32 s84, s84, 73728\n"
"  s_add_u32 s60, s53, 0\n"
"  s_and_b32 s60, s60, 7\n"
"  s_mul_i32 s68, s60, 196608\n"
"  s_lshl_b32 s60, s60, 7\n"
"  s_add_u32 s68, s68, s87\n"
"  s_add_u32 s76, s60, s86\n"
"  s_add_u32 s60, s53, 1\n"
"  s_and_b32 s60, s60, 7\n"
"  s_mul_i32 s69, s60, 196608\n"
"  s_lshl_b32 s60, s60, 7\n"
"  s_add_u32 s69, s69, s87\n"
"  s_add_u32 s77, s60, s86\n"
"  s_add_u32 m0, s84, 0\n"
"  s_waitcnt lgkmcnt(0)\n"
"  buffer_load_dwordx4 v230, s[8:11], s76 offen lds\n"
"  buffer_load_dwordx4 v231, s[8:11], s76 offen offset:1024 lds\n"
"  s_add_u32 m0, s84, 8192\n"
"  s_nop 0\n"
"  buffer_load_dwordx4 v230, s[8:11], s77 offen lds\n"
"  buffer_load_dwordx4 v231, s[8:11], s77 offen offset:1024 lds\n"
"  s_add_u32 m0, s85, 0\n"
"  s_nop 0\n"
"  buffer_load_dwordx4 v232, s[12:15], s68 offen lds\n"
"  buffer_load_dwordx4 v233, s[12:15], s68 offen offset:1024 lds\n"
"  buffer_load_dwordx4 v234, s[12:15], s68 offen offset:2048 lds\n"
"  buffer_load_dwordx4 v235, s[12:15], s68 offen offset:3072 lds\n"
"  s_setprio 1\n"
"  s_load_dwordx16 s[24:39], s[0:1], 0x40\n"
"  s_load_dwordx4 s[40:43], s[0:1], 0x80\n"
"  s_load_dword s57, s[20:21], 0x0\n"
"  s_lshr_b32 s50, s47, 2\n"
"  s_and_b32 s51, s47, 3\n"
"  s_lshl3_add_u32 s52, s46, s50\n"
"  s_lshr_b32 s55, s45, 1\n"
"  s_mov_b32 s59, 0xffff0000\n"
"  s_min_u32 s3, s46, 1\n"
"  s_andn2_b32 s3, s3, s47\n"
"  v_lshlrev_b32 v253, 2, v255\n"
"  v_lshl_or_b32 v207, v243, 10, v244\n"
"  s_add_u32 s60, s53, 2\n"
"  s_and_b32 s60, s60, 7\n"
"  s_mul_i32 s70, s60, 196608\n"
"  s_lshl_b32 s60, s60, 7\n"
"  s_add_u32 s70, s70, s87\n"
"  s_add_u32 s78, s60, s86\n"
"  s_add_u32 s60, s53, 3\n"
"  s_and_b32 s60, s60, 7\n"
"  s_mul_i32 s71, s60, 196608\n"
"  s_lshl_b32 s60, s60, 7\n"
"  s_add_u32 s71, s71, s87\n"
"  s_add_u32 s79, s60, s86\n"
"  s_add_u32 s60, s53, 4\n"
"  s_and_b32 s60, s60, 7\n"
"  s_mul_i32 s72, s60, 196608\n"
"  s_lshl_b32 s60, s60, 7\n"
"  s_add_u32 s72, s72, s87\n"
"  s_add_u32 s80, s60, s86\n"
"  s_add_u32 s60, s53, 5\n"
"  s_and_b32 s60, s60, 7\n"
"  s_mul_i32 s73, s60, 196608\n"
"  s_lshl_b32 s60, s60, 7\n"
"  s_add_u32 s73, s73, s87\n"
"  s_add_u32 s81, s60, s86\n"
"  s_add_u32 s60, s53, 6\n"
"  s_and_b32 s60, s60, 7\n"
"  s_mul_i32 s74, s60, 196608\n"
"  s_lshl_b32 s60, s60, 7\n"
"  s_add_u32 s74, s74, s87\n"
"  s_add_u32 s82, s60, s86\n"
"  s_add_u32 s60, s53, 7\n"
"  s_and_b32 s60, s60, 7\n"
"  s_mul_i32 s75, s60, 196608\n"
"  s_lshl_b32 s60, s60, 7\n"
"  s_add_u32 s75, s75, s87\n"
"  s_add_u32 s83, s60, s86\n"
"  s_mul_i32 s89, s50, 2\n"
"  s_and_b32 s60, s45, 1\n"
"  s_lshl_b32 s88, s60, 13\n"
"  s_lshl_b32 s60, s51, 8\n"
"  s_add_u32 s88, s88, s60\n"
"  s_lshl_b32 s60, s55, 7\n"
"  s_add_u32 s88, s88, s60\n"
"  v_bfe_u32 v243, v255, 2, 2\n"
"  v_lshlrev_b32 v204, 7, v243\n"
"  v_lshrrev_b32 v243, 1, v243\n"
"  v_lshl_or_b32 v204, v243, 6, v204\n"
"  v_bfe_u32 v243, v255, 4, 1\n"
"  v_lshl_or_b32 v204, v243, 5, v204\n"
"  v_and_b32 v243, 3, v255\n"
"  v_lshl_or_b32 v204, v243, 3, v204\n"
"  s_and_b32 s54, s45, 1\n"
"  s_xor_b32 s54, s54, 1\n"
"  v_add_u32 v236, s84, v254\n"
"  v_bfe_u32 v243, v255, 2, 2\n"
"  v_lshrrev_b32 v244, 4, v255\n"
"  v_cmp_eq_u32 vcc, v243, v244\n"
"  v_xor_b32 v243, v243, v244\n"
"  v_mov_b32 v244, 0x3f803f80\n"
"  v_and_b32 v243, 1, v243\n"
"  v_cndmask_b32 v52, 0, v244, vcc\n"
"  v_cmp_eq_u32 vcc, 0, v243\n"
"  s_nop 1\n"
"  v_cndmask_b32 v56, 0, v244, vcc\n"
"  v_mov_b32 v53, v52\n"
"  v_mov_b32 v57, v56\n"
"  v_mov_b32 v54, v52\n"
"  v_mov_b32 v58, v56\n"
"  v_mov_b32 v55, v52\n"
"  v_mov_b32 v59, v56\n"
"  s_lshl_b32 s60, s55, 12\n"
"  s_add_u32 s60, s60, 74752\n"
"  v_add_u32 v200, s60, v254\n"
"  v_and_b32 v243, 0x2f0, v254\n"
"  s_add_u32 s60, s60, 1280\n"
"  v_add_u32 v243, s60, v243\n"
"  v_and_b32 v244, 0xf0, v254\n"
"  v_add_u32 v244, 155648, v244\n"
"  v_and_b32 v202, 16, v255\n"
"  v_cmp_eq_u32 vcc, 0, v202\n"
"  v_xor_b32 v202, 16, v202\n"
"  v_lshlrev_b32 v202, 9, v202\n"
"  v_cndmask_b32 v203, v244, v243, vcc\n"
"  v_lshrrev_b32 v243, 5, v255\n"
"  v_and_b32 v205, 31, v255\n"
"  v_lshlrev_b32 v243, 2, v243\n"
"  v_sub_u32 v205, v205, v243\n"
"  v_mov_b32 v244, 4286578688\n"
"  v_cmp_gt_i32 s[90:91], 0, v205\n"
"  v_cmp_gt_i32 s[92:93], 1, v205\n"
"  v_nop\n"
"  v_cndmask_b32 v144, 0, v244, s[90:91]\n"
"  v_cndmask_b32 v145, 0, v244, s[92:93]\n"
"  v_xor_b32 v180, v244, v144\n"
"  v_xor_b32 v181, v244, v145\n"
"  v_cmp_gt_i32 s[90:91], 2, v205\n"
"  v_cmp_gt_i32 s[92:93], 3, v205\n"
"  v_nop\n"
"  v_cndmask_b32 v146, 0, v244, s[90:91]\n"
"  v_cndmask_b32 v147, 0, v244, s[92:93]\n"
"  v_xor_b32 v182, v244, v146\n"
"  v_xor_b32 v183, v244, v147\n"
"  v_cmp_gt_i32 s[90:91], 8, v205\n"
"  v_cmp_gt_i32 s[92:93], 9, v205\n"
"  v_nop\n"
"  v_cndmask_b32 v148, 0, v244, s[90:91]\n"
"  v_cndmask_b32 v149, 0, v244, s[92:93]\n"
"  v_xor_b32 v184, v244, v148\n"
"  v_xor_b32 v185, v244, v149\n"
"  v_cmp_gt_i32 s[90:91], 10, v205\n"
"  v_cmp_gt_i32 s[92:93], 11, v205\n"
"  v_nop\n"
"  v_cndmask_b32 v150, 0, v244, s[90:91]\n"
"  v_cndmask_b32 v151, 0, v244, s[92:93]\n"
"  v_xor_b32 v186, v244, v150\n"
"  v_xor_b32 v187, v244, v151\n"
"  v_cmp_gt_i32 s[90:91], 16, v205\n"
"  v_cmp_gt_i32 s[92:93], 17, v205\n"
"  v_nop\n"
"  v_cndmask_b32 v152, 0, v244, s[90:91]\n"
"  v_cndmask_b32 v153, 0, v244, s[92:93]\n"
"  v_xor_b32 v188, v244, v152\n"
"  v_xor_b32 v189, v244, v153\n"
"  v_cmp_gt_i32 s[90:91], 18, v205\n"
"  v_cmp_gt_i32 s[92:93], 19, v205\n"
"  v_nop\n"
"  v_cndmask_b32 v154, 0, v244, s[90:91]\n"
"  v_cndmask_b32 v155, 0, v244, s[92:93]\n"
"  v_xor_b32 v190, v244, v154\n"
"  v_xor_b32 v191, v244, v155\n"
"  v_cmp_gt_i32 s[90:91], 24, v205\n"
"  v_cmp_gt_i32 s[92:93], 25, v205\n"
"  v_nop\n"
"  v_cndmask_b32 v156, 0, v244, s[90:91]\n"
"  v_cndmask_b32 v157, 0, v244, s[92:93]\n"
"  v_xor_b32 v192, v244, v156\n"
"  v_xor_b32 v193, v244, v157\n"
"  v_cmp_gt_i32 s[90:91], 26, v205\n"
"  v_cmp_gt_i32 s[92:93], 27, v205\n"
"  v_nop\n"
"  v_cndmask_b32 v158, 0, v244, s[90:91]\n"
"  v_cndmask_b32 v159, 0, v244, s[92:93]\n"
"  v_xor_b32 v194, v244, v158\n"
"  v_xor_b32 v195, v244, v159\n"
"  v_lshrrev_b32 v243, 5, v255\n"
"  v_lshl_add_u32 v237, v243, 10, v204\n"
"  s_add_u32 s60, s85, 73728\n"
"  v_add_u32 v237, s60, v237\n"
"  v_mov_b32 v84, 0\n"
"  v_mov_b32 v92, 0\n"
"  v_xor_b32 v238, 64, v237\n"
"  v_add_u32 v239, 4096, v253\n"
"  v_and_b32 v243, 1, v255\n"
"  v_lshrrev_b32 v241, 3, v255\n"
"  v_lshl_or_b32 v243, v243, 3, v241\n"
"  v_lshl_or_b32 v243, s45, 4, v243\n"
"  v_lshlrev_b32 v240, 3, v243\n"
"  v_lshlrev_b32 v241, 2, v243\n"
"  v_add_u32 v240, 156928, v240\n"
"  v_add_u32 v241, 156416, v241\n"
"  s_lshl2_add_u32 s60, s45, 157696\n"
"  v_add_u32 v242, s60, v254\n"
"  s_lshl_b32 s60, s45, 10\n"
"  s_add_u32 s60, s60, 65536\n"
"  v_add_u32 v243, s60, v254\n"
"  s_cmp_lg_u32 s45, 0\n"
"  s_cbranch_scc1 .Lfb_L_mk0\n"
"  ds_write_b128 v243, v[144:147]\n"
"  ds_write_b128 v243, v[180:183] offset:4096\n"
".Lfb_L_mk0:\n"
"  s_cmp_lg_u32 s45, 1\n"
"  s_cbranch_scc1 .Lfb_L_mk1\n"
"  ds_write_b128 v243, v[148:151]\n"
"  ds_write_b128 v243, v[184:187] offset:4096\n"
".Lfb_L_mk1:\n"
"  s_cmp_lg_u32 s45, 2\n"
"  s_cbranch_scc1 .Lfb_L_mk2\n"
"  ds_write_b128 v243, v[152:155]\n"
"  ds_write_b128 v243, v[188:191] offset:4096\n"
".Lfb_L_mk2:\n"
"  s_cmp_lg_u32 s45, 3\n"
"  s_cbranch_scc1 .Lfb_L_mk3\n"
"  ds_write_b128 v243, v[156:159]\n"
"  ds_write_b128 v243, v[192:195] offset:4096\n"
".Lfb_L_mk3:\n"
"  s_add_u32 m0, s84, 16384\n"
"  s_nop 0\n"
"  buffer_load_dwordx4 v230, s[8:11], s78 offen lds\n"
"  buffer_load_dwordx4 v231, s[8:11], s78 offen offset:1024 lds\n"
"  s_add_u32 m0, s85, 16384\n"
"  s_nop 0\n"
"  buffer_load_dwordx4 v232, s[12:15], s69 offen lds\n"
"  buffer_load_dwordx4 v233, s[12:15], s69 offen offset:1024 lds\n"
"  buffer_load_dwordx4 v234, s[12:15], s69 offen offset:2048 lds\n"
"  buffer_load_dwordx4 v235, s[12:15], s69 offen offset:3072 lds\n"
"  s_waitcnt vmcnt(12)\n"
"  ds_read_b128 v[32:35], v236\n"
"  ds_read_b128 v[36:39], v236 offset:1024\n"
"  s_waitcnt lgkmcnt(1)\n"
"  v_mfma_f32_16x16x32_bf16 v[40:43], v[52:55], v[32:35], 0\n"
"  s_waitcnt lgkmcnt(0)\n"
"  v_mfma_f32_16x16x32_bf16 v[44:47], v[52:55], v[36:39], 0\n"
"  v_mfma_f32_4x4x4_16b_bf16 v[84:87], v[32:33], v[32:33], 0\n"
"  v_mfma_f32_4x4x4_16b_bf16 v[88:91], v[34:35], v[34:35], 0\n"
"  v_mfma_f32_4x4x4_16b_bf16 v[92:95], v[36:37], v[36:37], 0\n"
"  v_mfma_f32_4x4x4_16b_bf16 v[96:99], v[38:39], v[38:39], 0\n"
"  s_waitcnt vmcnt(6)\n"
"  ds_read_b128 v[32:35], v236 offset:8192\n"
"  ds_read_b128 v[36:39], v236 offset:9216\n"
"  s_barrier\n"
"  s_add_u32 m0, s84, 24576\n"
"  s_nop 0\n"
"  buffer_load_dwordx4 v230, s[8:11], s79 offen lds\n"
"  buffer_load_dwordx4 v231, s[8:11], s79 offen offset:1024 lds\n"
"  s_add_u32 m0, s85, 32768\n"
"  s_nop 0\n"
"  buffer_load_dwordx4 v232, s[12:15], s70 offen lds\n"
"  buffer_load_dwordx4 v233, s[12:15], s70 offen offset:1024 lds\n"
"  buffer_load_dwordx4 v234, s[12:15], s70 offen offset:2048 lds\n"
"  buffer_load_dwordx4 v235, s[12:15], s70 offen offset:3072 lds\n"
"  s_waitcnt lgkmcnt(1)\n"
"  v_mfma_f32_16x16x32_bf16 v[40:43], v[52:55], v[32:35], v[40:43]\n"
"  s_waitcnt lgkmcnt(0)\n"
"  v_mfma_f32_16x16x32_bf16 v[44:47], v[52:55], v[36:39], v[44:47]\n"
"  v_mfma_f32_4x4x4_16b_bf16 v[84:87], v[32:33], v[32:33], v[84:87]\n"
"  v_mfma_f32_4x4x4_16b_bf16 v[88:91], v[34:35], v[34:35], v[88:91]\n"
"  v_mfma_f32_4x4x4_16b_bf16 v[92:95], v[36:37], v[36:37], v[92:95]\n"
"  v_mfma_f32_4x4x4_16b_bf16 v[96:99], v[38:39], v[38:39], v[96:99]\n"
"  s_waitcnt vmcnt(6)\n"
"  ds_read_b128 v[32:35], v236 offset:16384\n"
"  ds_read_b128 v[36:39], v236 offset:17408\n"
"  s_barrier\n"
"  s_add_u32 m0, s84, 32768\n"
"  s_nop 0\n"
"  buffer_load_dwordx4 v230, s[8:11], s80 offen lds\n"
"  buffer_load_dwordx4 v231, s[8:11], s80 offen offset:1024 lds\n"
"  s_add_u32 m0, s85, 49152\n"
"  s_nop 0\n"
"  buffer_load_dwordx4 v232, s[12:15], s71 offen lds\n"
"  buffer_load_dwordx4 v233, s[12:15], s71 offen offset:1024 lds\n"
"  buffer_load_dwordx4 v234, s[12:15], s71 offen offset:2048 lds\n"
"  buffer_load_dwordx4 v235, s[12:15], s71 offen offset:3072 lds\n"
"  s_waitcnt lgkmcnt(1)\n"
"  v_mfma_f32_16x16x32_bf16 v[40:43], v[52:55], v[32:35], v[40:43]\n"
"  s_waitcnt lgkmcnt(0)\n"
"  v_mfma_f32_16x16x32_bf16 v[44:47], v[52:55], v[36:39], v[44:47]\n"
"  v_mfma_f32_4x4x4_16b_bf16 v[84:87], v[32:33], v[32:33], v[84:87]\n"
"  v_mfma_f32_4x4x4_16b_bf16 v[88:91], v[34:35], v[34:35], v[88:91]\n"
"  v_mfma_f32_4x4x4_16b_bf16 v[92:95], v[36:37], v[36:37], v[92:95]\n"
"  v_mfma_f32_4x4x4_16b_bf16 v[96:99], v[38:39], v[38:39], v[96:99]\n"
"  s_waitcnt vmcnt(6)\n"
"  ds_read_b128 v[32:35], v236 offset:24576\n"
"  ds_read_b128 v[36:39], v236 offset:25600\n"
"  s_barrier\n"
"  s_add_u32 m0, s84, 40960\n"
"  s_nop 0\n"
"  buffer_load_dwordx4 v230, s[8:11], s81 offen lds\n"
"  buffer_load_dwordx4 v231, s[8:11], s81 offen offset:1024 lds\n"
"  s_add_u32 m0, s85, 0\n"
"  s_nop 0\n"
"  buffer_load_dwordx4 v232, s[12:15], s72 offen lds\n"
"  buffer_load_dwordx4 v233, s[12:15], s72 offen offset:1024 lds\n"
"  buffer_load_dwordx4 v234, s[12:15], s72 offen offset:2048 lds\n"
"  buffer_load_dwordx4 v235, s[12:15], s72 offen offset:3072 lds\n"
"  s_waitcnt lgkmcnt(1)\n"
"  v_mfma_f32_16x16x32_bf16 v[40:43], v[52:55], v[32:35], v[40:43]\n"
"  s_waitcnt lgkmcnt(0)\n"
"  v_mfma_f32_16x16x32_bf16 v[44:47], v[52:55], v[36:39], v[44:47]\n"
"  v_mfma_f32_4x4x4_16b_bf16 v[84:87], v[32:33], v[32:33], v[84:87]\n"
"  v_mfma_f32_4x4x4_16b_bf16 v[88:91], v[34:35], v[34:35], v[88:91]\n"
"  v_mfma_f32_4x4x4_16b_bf16 v[92:95], v[36:37], v[36:37], v[92:95]\n"
"  v_mfma_f32_4x4x4_16b_bf16 v[96:99], v[38:39], v[38:39], v[96:99]\n"
"  s_waitcnt vmcnt(6)\n"
"  ds_read_b128 v[32:35], v236 offset:32768\n"
"  ds_read_b128 v[36:39], v236 offset:33792\n"
"  s_barrier\n"
"  s_add_u32 m0, s84, 49152\n"
"  s_nop 0\n"
"  buffer_load_dwordx4 v230, s[8:11], s82 offen lds\n"
"  buffer_load_dwordx4 v231, s[8:11], s82 offen offset:1024 lds\n"
"  s_add_u32 m0, s85, 16384\n"
"  s_nop 0\n"
"  buffer_load_dwordx4 v232, s[12:15], s73 offen lds\n"
"  buffer_load_dwordx4 v233, s[12:15], s73 offen offset:1024 lds\n"
"  buffer_load_dwordx4 v234, s[12:15], s73 offen offset:2048 lds\n"
"  buffer_load_dwordx4 v235, s[12:15], s73 offen offset:3072 lds\n"
"  s_waitcnt lgkmcnt(1)\n"
"  v_mfma_f32_16x16x32_bf16 v[40:43], v[52:55], v[32:35], v[40:43]\n"
"  s_waitcnt lgkmcnt(0)\n"
"  v_mfma_f32_16x16x32_bf16 v[44:47], v[52:55], v[36:39], v[44:47]\n"
"  v_mfma_f32_4x4x4_16b_bf16 v[84:87], v[32:33], v[32:33], v[84:87]\n"
"  v_mfma_f32_4x4x4_16b_bf16 v[88:91], v[34:35], v[34:35], v[88:91]\n"
"  v_mfma_f32_4x4x4_16b_bf16 v[92:95], v[36:37], v[36:37], v[92:95]\n"
"  v_mfma_f32_4x4x4_16b_bf16 v[96:99], v[38:39], v[38:39], v[96:99]\n"
"  s_waitcnt vmcnt(6)\n"
"  ds_read_b128 v[32:35], v236 offset:40960\n"
"  ds_read_b128 v[36:39], v236 offset:41984\n"
"  s_barrier\n"
"  s_add_u32 m0, s84, 57344\n"
"  s_nop 0\n"
"  buffer_load_dwordx4 v230, s[8:11], s83 offen lds\n"
"  buffer_load_dwordx4 v231, s[8:11], s83 offen offset:1024 lds\n"
"  s_add_u32 m0, s85, 32768\n"
"  s_nop 0\n"
"  buffer_load_dwordx4 v232, s[12:15], s74 offen lds\n"
"  buffer_load_dwordx4 v233, s[12:15], s74 offen offset:1024 lds\n"
"  buffer_load_dwordx4 v234, s[12:15], s74 offen offset:2048 lds\n"
"  buffer_load_dwordx4 v235, s[12:15], s74 offen offset:3072 lds\n"
"  s_waitcnt lgkmcnt(1)\n"
"  v_mfma_f32_16x16x32_bf16 v[40:43], v[52:55], v[32:35], v[40:43]\n"
"  s_waitcnt lgkmcnt(0)\n"
"  v_mfma_f32_16x16x32_bf16 v[44:47], v[52:55], v[36:39], v[44:47]\n"
"  v_mfma_f32_4x4x4_16b_bf16 v[84:87], v[32:33], v[32:33], v[84:87]\n"
"  v_mfma_f32_4x4x4_16b_bf16 v[88:91], v[34:35], v[34:35], v[88:91]\n"
"  v_mfma_f32_4x4x4_16b_bf16 v[92:95], v[36:37], v[36:37], v[92:95]\n"
"  v_mfma_f32_4x4x4_16b_bf16 v[96:99], v[38:39], v[38:39], v[96:99]\n"
"  s_waitcnt vmcnt(6)\n"
"  ds_read_b128 v[32:35], v236 offset:49152\n"
"  ds_read_b128 v[36:39], v236 offset:50176\n"
"  s_barrier\n"
"  s_add_u32 m0, s85, 49152\n"
"  s_nop 0\n"
"  buffer_load_dwordx4 v232, s[12:15], s75 offen lds\n"
"  buffer_load_dwordx4 v233, s[12:15], s75 offen offset:1024 lds\n"
"  buffer_load_dwordx4 v234, s[12:15], s75 offen offset:2048 lds\n"
"  buffer_load_dwordx4 v235, s[12:15], s75 offen offset:3072 lds\n"
"  s_add_u32 m0, s85, 73728\n"
"  s_nop 0\n"
"  buffer_load_dwordx4 v234, s[12:15], s68 offen lds\n"
"  buffer_load_dwordx4 v235, s[12:15], s68 offen offset:1024 lds\n"
"  s_add_u32 m0, s85, 75776\n"
"  s_nop 0\n"
"  buffer_load_dwordx4 v234, s[12:15], s69 offen lds\n"
"  buffer_load_dwordx4 v235, s[12:15], s69 offen offset:1024 lds\n"
"  s_waitcnt lgkmcnt(1)\n"
"  v_mfma_f32_16x16x32_bf16 v[40:43], v[52:55], v[32:35], v[40:43]\n"
"  s_waitcnt lgkmcnt(0)\n"
"  v_mfma_f32_16x16x32_bf16 v[44:47], v[52:55], v[36:39], v[44:47]\n"
"  v_mfma_f32_4x4x4_16b_bf16 v[84:87], v[32:33], v[32:33], v[84:87]\n"
"  v_mfma_f32_4x4x4_16b_bf16 v[88:91], v[34:35], v[34:35], v[88:91]\n"
"  v_mfma_f32_4x4x4_16b_bf16 v[92:95], v[36:37], v[36:37], v[92:95]\n"
"  v_mfma_f32_4x4x4_16b_bf16 v[96:99], v[38:39], v[38:39], v[96:99]\n"
"  s_waitcnt vmcnt(8)\n"
"  ds_read_b128 v[32:35], v236 offset:57344\n"
"  ds_read_b128 v[36:39], v236 offset:58368\n"
"  s_barrier\n"
"  s_add_u32 m0, s85, 90112\n"
"  s_nop 0\n"
"  buffer_load_dwordx4 v234, s[12:15], s70 offen lds\n"
"  buffer_load_dwordx4 v235, s[12:15], s70 offen offset:1024 lds\n"
"  s_add_u32 m0, s85, 92160\n"
"  s_nop 0\n"
"  buffer_load_dwordx4 v234, s[12:15], s71 offen lds\n"
"  buffer_load_dwordx4 v235, s[12:15], s71 offen offset:1024 lds\n"
"  s_waitcnt lgkmcnt(1)\n"
"  v_mfma_f32_16x16x32_bf16 v[40:43], v[52:55], v[32:35], v[40:43]\n"
"  s_waitcnt lgkmcnt(0)\n"
"  v_mfma_f32_16x16x32_bf16 v[44:47], v[52:55], v[36:39], v[44:47]\n"
"  v_mfma_f32_4x4x4_16b_bf16 v[84:87], v[32:33], v[32:33], v[84:87]\n"
"  v_mfma_f32_4x4x4_16b_bf16 v[88:91], v[34:35], v[34:35], v[88:91]\n"
"  v_mfma_f32_4x4x4_16b_bf16 v[92:95], v[36:37], v[36:37], v[92:95]\n"
"  v_mfma_f32_4x4x4_16b_bf16 v[96:99], v[38:39], v[38:39], v[96:99]\n"
"  s_mov_b32 vcc_lo, 0xaaaaaaaa\n"
"  s_mov_b32 vcc_hi, 0xaaaaaaaa\n"
"  s_mov_b32 s90, 0xcccccccc\n"
"  s_mov_b32 s91, 0xcccccccc\n"
"  s_mov_b32 s60, 0x3b000000\n"
"  v_pk_add_f32 v[84:85], v[84:85], v[88:89]\n"
"  v_pk_add_f32 v[86:87], v[86:87], v[90:91]\n"
"  v_pk_add_f32 v[92:93], v[92:93], v[96:97]\n"
"  v_pk_add_f32 v[94:95], v[94:95], v[98:99]\n"
"  v_cndmask_b32 v84, v84, v85, vcc\n"
"  v_cndmask_b32 v86, v86, v87, vcc\n"
"  v_cndmask_b32 v92, v92, v93, vcc\n"
"  v_cndmask_b32 v94, v94, v95, vcc\n"
"  v_cndmask_b32 v84, v84, v86, s[90:91]\n"
"  v_cndmask_b32 v92, v92, v94, s[90:91]\n"
"  s_waitcnt vmcnt(8)\n"
"  s_barrier\n"
"  s_add_u32 m0, s85, 0\n"
"  s_nop 0\n"
"  buffer_load_dwordx4 v234, s[12:15], s72 offen lds\n"
"  buffer_load_dwordx4 v235, s[12:15], s72 offen offset:1024 lds\n"
"  s_add_u32 m0, s85, 2048\n"
"  s_nop 0\n"
"  buffer_load_dwordx4 v234, s[12:15], s73 offen lds\n"
"  buffer_load_dwordx4 v235, s[12:15], s73 offen offset:1024 lds\n"
"  v_add_f32_dpp v40, v40, v40 quad_perm:[1,0,3,2] row_mask:0xf bank_mask:0xf\n"
"  v_add_f32_dpp v44, v44, v44 quad_perm:[1,0,3,2] row_mask:0xf bank_mask:0xf\n"
"  v_add_f32_dpp v84, v84, v84 quad_perm:[1,0,3,2] row_mask:0xf bank_mask:0xf\n"
"  v_add_f32_dpp v92, v92, v92 quad_perm:[1,0,3,2] row_mask:0xf bank_mask:0xf\n"
"  v_add_f32_dpp v40, v40, v40 quad_perm:[2,3,0,1] row_mask:0xf bank_mask:0xf\n"
"  v_add_f32_dpp v44, v44, v44 quad_perm:[2,3,0,1] row_mask:0xf bank_mask:0xf\n"
"  v_add_f32_dpp v84, v84, v84 quad_perm:[2,3,0,1] row_mask:0xf bank_mask:0xf\n"
"  v_add_f32_dpp v92, v92, v92 quad_perm:[2,3,0,1] row_mask:0xf bank_mask:0xf\n"
"  v_add_f32_dpp v40, v40, v40 row_half_mirror row_mask:0xf bank_mask:0xf\n"
"  v_add_f32_dpp v44, v44, v44 row_half_mirror row_mask:0xf bank_mask:0xf\n"
"  v_add_f32_dpp v84, v84, v84 row_half_mirror row_mask:0xf bank_mask:0xf\n"
"  v_add_f32_dpp v92, v92, v92 row_half_mirror row_mask:0xf bank_mask:0xf\n"
"  v_cndmask_b32 v40, v40, v44, vcc\n"
"  v_cndmask_b32 v84, v84, v92, vcc\n"
"  v_mul_f32 v40, s60, v40\n"
"  v_mul_f32 v50, v40, v40\n"
"  v_cvt_pk_bf16_f32 v51, v40, v40\n"
"  v_fma_f32 v50, v84, s60, -v50\n"
"  v_lshlrev_b32 v51, 16, v51\n"
"  v_max_f32 v50, 0, v50\n"
"  v_sub_f32 v51, v40, v51\n"
"  v_add_f32 v50, 0x3727c5ac, v50\n"
"  v_cvt_pk_bf16_f32 v51, v40, v51\n"
"  v_rsq_f32 v50, v50\n"
"  ds_write2_b32 v240, v51, v51 offset0:0 offset1:1\n"
"  ds_write_b32 v241, v50\n"
"  s_waitcnt lgkmcnt(0)\n"
"  s_barrier\n"
"  s_add_u32 m0, s85, 16384\n"
"  s_nop 0\n"
"  buffer_load_dwordx4 v234, s[12:15], s74 offen lds\n"
"  buffer_load_dwordx4 v235, s[12:15], s74 offen offset:1024 lds\n"
"  s_add_u32 m0, s85, 18432\n"
"  s_nop 0\n"
"  buffer_load_dwordx4 v234, s[12:15], s75 offen lds\n"
"  buffer_load_dwordx4 v235, s[12:15], s75 offen offset:1024 lds\n"
"  s_barrier\n"
"  s_add_u32 s56, s57, 1\n"
"  s_mul_i32 s56, s56, 0x9E3779B1\n"
"  s_add_u32 s56, s56, 0x7F4A7C15\n"
"  s_mov_b32 s58, 0\n"
"  s_and_b32 s60, s45, 1\n"
"  s_lshl1_add_u32 s61, s49, s60\n"
"  s_lshl_b32 s61, s61, 5\n"
"  s_add_u32 s61, s61, s48\n"
"  s_lshl1_add_u32 s61, s61, s55\n"
"  s_mul_i32 s61, s61, 4352\n"
"  s_add_u32 s80, s85, 73728\n"
"  v_add_u32 v245, s80, v254\n"
"  s_mul_i32 s62, s48, 43\n"
"  s_lshr_b32 s62, s62, 7\n"
"  s_mul_i32 s62, s62, 3\n"
"  s_sub_u32 s62, s48, s62\n"
"  s_cmp_lt_u32 s48, 3\n"
"  s_cselect_b32 s62, 0, s62\n"
"  s_mul_i32 s60, s62, 8704\n"
"  s_sub_u32 s76, s61, s60\n"
"  s_add_u32 s76, s76, 1039872\n"
"  s_add_u32 s62, s62, 1\n"
"  s_cmp_eq_u32 s62, 3\n"
"  s_cselect_b32 s62, 0, s62\n"
"  s_cmp_gt_u32 s48, 0\n"
"  s_cselect_b32 s76, s76, 524288\n"
"  s_mul_i32 s60, s62, 8704\n"
"  s_sub_u32 s77, s61, s60\n"
"  s_add_u32 s77, s77, 1039872\n"
"  s_add_u32 s62, s62, 1\n"
"  s_cmp_eq_u32 s62, 3\n"
"  s_cselect_b32 s62, 0, s62\n"
"  s_cmp_gt_u32 s48, 1\n"
"  s_cselect_b32 s77, s77, 524288\n"
"  s_mul_i32 s60, s62, 8704\n"
"  s_sub_u32 s78, s61, s60\n"
"  s_add_u32 s78, s78, 1039872\n"
"  s_add_u32 s62, s62, 1\n"
"  s_cmp_eq_u32 s62, 3\n"
"  s_cselect_b32 s62, 0, s62\n"
"  s_cmp_gt_u32 s48, 2\n"
"  s_cselect_b32 s78, s78, 524288\n"
"  s_add_u32 s79, s61, 1013760\n"
"  s_cmp_gt_u32 s48, 3\n"
"  s_cselect_b32 s79, s79, 524288\n"
"  s_waitcnt vmcnt(8)\n"
"  ds_read_b64_tr_b16 v[0:1], v237\n"
"  ds_read_b64_tr_b16 v[2:3], v237 offset:512\n"
"  ds_read_b64_tr_b16 v[4:5], v238\n"
"  ds_read_b64_tr_b16 v[6:7], v238 offset:512\n"
"  ds_read_b64_tr_b16 v[8:9], v237 offset:2048\n"
"  ds_read_b64_tr_b16 v[10:11], v237 offset:2560\n"
"  ds_read_b64_tr_b16 v[12:13], v238 offset:2048\n"
"  ds_read_b64_tr_b16 v[14:15], v238 offset:2560\n"
"  ds_read_b64_tr_b16 v[16:17], v237 offset:16384\n"
"  ds_read_b64_tr_b16 v[18:19], v237 offset:16896\n"
"  ds_read_b64_tr_b16 v[20:21], v238 offset:16384\n"
"  ds_read_b64_tr_b16 v[22:23], v238 offset:16896\n"
"  ds_read_b64_tr_b16 v[24:25], v237 offset:18432\n"
"  ds_read_b64_tr_b16 v[26:27], v237 offset:18944\n"
"  ds_read_b64_tr_b16 v[28:29], v238 offset:18432\n"
"  ds_read_b64_tr_b16 v[30:31], v238 offset:18944\n"
"  s_waitcnt lgkmcnt(14)\n"
"  v_mfma_f32_16x16x32_bf16 v[60:63], v[56:59], v[0:3], 0\n"
"  s_waitcnt lgkmcnt(12)\n"
"  v_mfma_f32_16x16x32_bf16 v[64:67], v[56:59], v[4:7], 0\n"
"  s_waitcnt lgkmcnt(10)\n"
"  v_mfma_f32_16x16x32_bf16 v[60:63], v[56:59], v[8:11], v[60:63]\n"
"  s_waitcnt lgkmcnt(8)\n"
"  v_mfma_f32_16x16x32_bf16 v[64:67], v[56:59], v[12:15], v[64:67]\n"
"  s_waitcnt lgkmcnt(6)\n"
"  v_mfma_f32_16x16x32_bf16 v[60:63], v[56:59], v[16:19], v[60:63]\n"
"  s_waitcnt lgkmcnt(4)\n"
"  v_mfma_f32_16x16x32_bf16 v[64:67], v[56:59], v[20:23], v[64:67]\n"
"  s_waitcnt lgkmcnt(2)\n"
"  v_mfma_f32_16x16x32_bf16 v[60:63], v[56:59], v[24:27], v[60:63]\n"
"  s_waitcnt lgkmcnt(0)\n"
"  v_mfma_f32_16x16x32_bf16 v[64:67], v[56:59], v[28:31], v[64:67]\n"
"  v_subrev_u32 v237, 73728, v237\n"
"  v_subrev_u32 v238, 73728, v238\n"
"  s_mov_b32 vcc_lo, 0\n"
"  s_mov_b32 vcc_hi, -1\n"
"  s_waitcnt vmcnt(0)\n"
"  s_barrier\n"
"  s_add_u32 s60, s89, 0\n"
"  s_and_b32 s60, s60, 7\n"
"  s_lshl_b32 s60, s60, 16\n"
"  s_lshl_b32 s61, s45, 10\n"
"  s_add_u32 s60, s60, s88\n"
"  s_add_u32 m0, s61, 32768\n"
"  s_nop 0\n"
"  buffer_load_dwordx4 v207, s[16:19], s60 offen lds\n"
"  s_add_u32 m0, s61, 36864\n"
"  s_add_u32 s60, s60, 16384\n"
"  buffer_load_dwordx4 v207, s[16:19], s60 offen lds\n"
"  s_add_u32 m0, s61, 40960\n"
"  s_add_u32 s60, s60, 16384\n"
"  buffer_load_dwordx4 v207, s[16:19], s60 offen lds\n"
"  s_add_u32 m0, s61, 45056\n"
"  s_add_u32 s60, s60, 16384\n"
"  buffer_load_dwordx4 v207, s[16:19], s60 offen lds\n"
"  ds_read_b64_tr_b16 v[0:1], v237\n"
"  ds_read_b64_tr_b16 v[2:3], v237 offset:512\n"
"  ds_read_b64_tr_b16 v[4:5], v238\n"
"  ds_read_b64_tr_b16 v[6:7], v238 offset:512\n"
"  ds_read_b64_tr_b16 v[8:9], v237 offset:2048\n"
"  ds_read_b64_tr_b16 v[10:11], v237 offset:2560\n"
"  ds_read_b64_tr_b16 v[12:13], v238 offset:2048\n"
"  ds_read_b64_tr_b16 v[14:15], v238 offset:2560\n"
"  ds_read_b64_tr_b16 v[16:17], v237 offset:16384\n"
"  ds_read_b64_tr_b16 v[18:19], v237 offset:16896\n"
"  ds_read_b64_tr_b16 v[20:21], v238 offset:16384\n"
"  ds_read_b64_tr_b16 v[22:23], v238 offset:16896\n"
"  ds_read_b64_tr_b16 v[24:25], v237 offset:18432\n"
"  ds_read_b64_tr_b16 v[26:27], v237 offset:18944\n"
"  ds_read_b64_tr_b16 v[28:29], v238 offset:18432\n"
"  ds_read_b64_tr_b16 v[30:31], v238 offset:18944\n"
"  s_waitcnt lgkmcnt(14)\n"
"  v_mfma_f32_16x16x32_bf16 v[60:63], v[56:59], v[0:3], v[60:63]\n"
"  s_waitcnt lgkmcnt(12)\n"
"  v_mfma_f32_16x16x32_bf16 v[64:67], v[56:59], v[4:7], v[64:67]\n"
"  s_waitcnt lgkmcnt(10)\n"
"  v_mfma_f32_16x16x32_bf16 v[60:63], v[56:59], v[8:11], v[60:63]\n"
"  s_waitcnt lgkmcnt(8)\n"
"  v_mfma_f32_16x16x32_bf16 v[64:67], v[56:59], v[12:15], v[64:67]\n"
"  s_waitcnt lgkmcnt(6)\n"
"  v_mfma_f32_16x16x32_bf16 v[60:63], v[56:59], v[16:19], v[60:63]\n"
"  s_waitcnt lgkmcnt(4)\n"
"  v_mfma_f32_16x16x32_bf16 v[64:67], v[56:59], v[20:23], v[64:67]\n"
"  s_waitcnt lgkmcnt(2)\n"
"  v_mfma_f32_16x16x32_bf16 v[60:63], v[56:59], v[24:27], v[60:63]\n"
"  s_waitcnt lgkmcnt(0)\n"
"  v_mfma_f32_16x16x32_bf16 v[64:67], v[56:59], v[28:31], v[64:67]\n"
"  s_nop 7\n"
"  v_cndmask_b32 v60, v60, v64, vcc\n"
"  ds_write_b32 v242, v60\n"
"  s_waitcnt lgkmcnt(0)\n"
"  s_barrier\n"
"  s_add_u32 m0, s80, 49152\n"
"  buffer_load_dword v132, v239, s[20:23], s76 offen sc1\n"
"  buffer_load_dwordx4 v254, s[20:23], s76 offen sc1 lds\n"
"  buffer_load_dwordx4 v254, s[20:23], s76 offen offset:1024 sc1 lds\n"
"  buffer_load_dwordx4 v254, s[20:23], s76 offen offset:2048 sc1 lds\n"
"  buffer_load_dwordx4 v254, s[20:23], s76 offen offset:3072 sc1 lds\n"
"  s_mov_b64 s[4:5], s[32:33]\n"
"  s_mov_b64 s[6:7], s[34:35]\n"
"  v_lshrrev_b32 v243, 3, v255\n"
"  v_and_b32 v247, 0x70, v254\n"
"  v_lshl_or_b32 v247, v243, 10, v247\n"
"  s_sub_u32 s60, s46, 1\n"
"  s_lshl_b32 s60, s60, 5\n"
"  s_add_u32 s60, s60, s47\n"
"  s_lshr_b32 s60, s60, 1\n"
"  s_lshl_b32 s60, s60, 7\n"
"  s_add_u32 s60, s60, s44\n"
"  s_lshl_b32 s73, s60, 7\n"
"  s_add_u32 s74, s73, 8192\n"
"  s_cmp_eq_u32 s3, 0\n"
"  s_cbranch_scc0 .Lfb_L_wdp\n"
"  s_mov_b64 s[4:5], s[24:25]\n"
"  s_mov_b64 s[6:7], s[26:27]\n"
"  s_mov_b64 s[32:33], s[28:29]\n"
"  s_mov_b64 s[34:35], s[30:31]\n"
"  v_lshrrev_b32 v243, 4, v255\n"
"  v_mul_u32_u24 v247, 3584, v243\n"
"  v_bfe_u32 v243, v255, 2, 2\n"
"  v_lshl_add_u32 v247, v243, 7, v247\n"
"  v_and_b32 v243, 0x30, v254\n"
"  v_add_u32 v247, v247, v243\n"
"  s_lshr_b32 s60, s47, 1\n"
"  s_lshl3_add_u32 s60, s60, s44\n"
"  s_mul_i32 s60, s60, 14336\n"
"  s_lshl_b32 s73, s46, 9\n"
"  s_add_u32 s73, s73, s60\n"
"  s_sub_u32 s73, s73, 512\n"
"  s_mov_b32 s74, s73\n"
".Lfb_L_wdp:\n"
"  s_cmp_eq_u32 s46, 0\n"
"  s_cselect_b32 s6, 0, s6\n"
"  s_cselect_b32 s34, 0, s34\n"
"  s_cselect_b32 s73, 0, s73\n"
"  s_cselect_b32 s74, 0, s74\n"
"  s_lshl1_add_u32 s60, s89, s45\n"
"  s_add_u32 s60, s60, 0\n"
"  s_and_b32 s60, s60, 15\n"
"  s_lshl4_add_u32 s60, s52, s60\n"
"  s_mul_i32 s60, s60, 2560\n"
"  s_add_u32 s68, s60, 6291456\n"
"  s_lshl1_add_u32 s60, s89, s45\n"
"  s_add_u32 s60, s60, 4\n"
"  s_and_b32 s60, s60, 15\n"
"  s_lshl4_add_u32 s60, s52, s60\n"
"  s_mul_i32 s60, s60, 2560\n"
"  s_add_u32 s69, s60, 6291456\n"
"  s_lshl1_add_u32 s60, s89, s45\n"
"  s_add_u32 s60, s60, 8\n"
"  s_and_b32 s60, s60, 15\n"
"  s_lshl4_add_u32 s60, s52, s60\n"
"  s_mul_i32 s60, s60, 2560\n"
"  s_add_u32 s70, s60, 6291456\n"
"  s_lshl1_add_u32 s60, s89, s45\n"
"  s_add_u32 s60, s60, 12\n"
"  s_and_b32 s60, s60, 15\n"
"  s_lshl4_add_u32 s60, s52, s60\n"
"  s_mul_i32 s60, s60, 2560\n"
"  s_add_u32 s71, s60, 6291456\n"
"  s_xor_b32 s60, s45, 1\n"
"  s_lshl_b32 s60, s60, 10\n"
"  s_add_u32 s60, s60, 159744\n"
"  v_add_u32 v246, s60, v254\n"
"  s_add_u32 s60, s54, 8\n"
"  s_lshl_b32 s61, s60, 13\n"
"  v_add_u32 v243, s61, v200\n"
"  v_mad_u32_u24 v201, v202, s60, v203\n"
"  v_add_u32 v244, 0x10000, v200\n"
"  v_mov_b64 v[64:65], 0\n"
"  v_mov_b64 v[66:67], 0\n"
"  v_mov_b64 v[68:69], 0\n"
"  v_mov_b64 v[70:71], 0\n"
"  v_mov_b64 v[72:73], 0\n"
"  v_mov_b64 v[74:75], 0\n"
"  v_mov_b64 v[76:77], 0\n"
"  v_mov_b64 v[78:79], 0\n"
"  s_barrier\n"
"  ds_read_b128 v[196:199], v246\n"
"  ds_read_b128 v[80:83], v243\n"
"  ds_read_b128 v[84:87], v201\n"
"  ds_read_b128 v[88:91], v201 offset:1024\n"
"  v_mad_u32_u24 v201, v202, 8, v203\n"
"  s_waitcnt lgkmcnt(2)\n"
"  v_mfma_f32_32x32x16_bf16 v[32:47], v[80:83], v[196:199], v[144:159]\n"
"  ds_read_b128 v[80:83], v244\n"
"  ds_read_b128 v[92:95], v201\n"
"  ds_read_b128 v[96:99], v201 offset:1024\n"
"  s_cmp_eq_u32 s54, 0\n"
"  s_nop 7\n"
"  v_exp_f32 v32, v32\n"
"  v_exp_f32 v33, v33\n"
"  v_exp_f32 v34, v34\n"
"  v_exp_f32 v35, v35\n"
"  v_cvt_pk_bf16_f32 v32, v32, v33\n"
"  v_exp_f32 v36, v36\n"
"  v_exp_f32 v37, v37\n"
"  v_cvt_pk_bf16_f32 v33, v34, v35\n"
"  v_exp_f32 v38, v38\n"
"  v_exp_f32 v39, v39\n"
"  v_cvt_pk_bf16_f32 v34, v36, v37\n"
"  v_exp_f32 v40, v40\n"
"  v_cvt_pk_bf16_f32 v35, v38, v39\n"
"  v_exp_f32 v41, v41\n"
"  v_exp_f32 v42, v42\n"
"  s_waitcnt lgkmcnt(4)\n"
"  v_mfma_f32_32x32x16_bf16 v[64:79], v[84:87], v[32:35], v[64:79]\n"
"  v_exp_f32 v43, v43\n"
"  v_cvt_pk_bf16_f32 v40, v40, v41\n"
"  s_waitcnt lgkmcnt(2)\n"
"  v_mfma_f32_32x32x16_bf16 v[48:63], v[80:83], v[196:199], 0\n"
"  v_exp_f32 v44, v44\n"
"  v_exp_f32 v45, v45\n"
"  v_cvt_pk_bf16_f32 v41, v42, v43\n"
"  v_exp_f32 v46, v46\n"
"  v_exp_f32 v47, v47\n"
"  v_cvt_pk_bf16_f32 v42, v44, v45\n"
"  v_cvt_pk_bf16_f32 v43, v46, v47\n"
"  s_nop 1\n"
"  v_mfma_f32_32x32x16_bf16 v[64:79], v[88:91], v[40:43], v[64:79]\n"
"  s_cbranch_scc1 .Lfb_L_own\n"
"  s_nop 0\n"
"  v_exp_f32 v48, v48\n"
"  v_exp_f32 v49, v49\n"
"  v_exp_f32 v50, v50\n"
"  v_exp_f32 v51, v51\n"
"  v_cvt_pk_bf16_f32 v48, v48, v49\n"
"  v_exp_f32 v52, v52\n"
"  v_exp_f32 v53, v53\n"
"  v_cvt_pk_bf16_f32 v49, v50, v51\n"
"  v_exp_f32 v54, v54\n"
"  v_exp_f32 v55, v55\n"
"  v_cvt_pk_bf16_f32 v50, v52, v53\n"
"  v_exp_f32 v56, v56\n"
"  v_cvt_pk_bf16_f32 v51, v54, v55\n"
"  v_exp_f32 v57, v57\n"
"  v_exp_f32 v58, v58\n"
"  s_waitcnt lgkmcnt(1)\n"
"  v_mfma_f32_32x32x16_bf16 v[64:79], v[92:95], v[48:51], v[64:79]\n"
"  v_exp_f32 v59, v59\n"
"  v_cvt_pk_bf16_f32 v56, v56, v57\n"
"  v_exp_f32 v60, v60\n"
"  v_exp_f32 v61, v61\n"
"  v_cvt_pk_bf16_f32 v57, v58, v59\n"
"  v_exp_f32 v62, v62\n"
"  v_exp_f32 v63, v63\n"
"  v_cvt_pk_bf16_f32 v58, v60, v61\n"
"  v_cvt_pk_bf16_f32 v59, v62, v63\n"
"  s_waitcnt lgkmcnt(0)\n"
"  s_nop 0\n"
"  v_mfma_f32_32x32x16_bf16 v[64:79], v[96:99], v[56:59], v[64:79]\n"
".Lfb_L_own:\n"
"  s_add_u32 m0, s80, 32768\n"
"  buffer_load_dword v133, v239, s[20:23], s77 offen sc1\n"
"  buffer_load_dwordx4 v254, s[20:23], s77 offen sc1 lds\n"
"  buffer_load_dwordx4 v254, s[20:23], s77 offen offset:1024 sc1 lds\n"
"  buffer_load_dwordx4 v254, s[20:23], s77 offen offset:2048 sc1 lds\n"
"  buffer_load_dwordx4 v254, s[20:23], s77 offen offset:3072 sc1 lds\n"
"  s_waitcnt vmcnt(5)\n"
"  ds_read_b128 v[100:103], v245 offset:49152\n"
"  ds_read_b128 v[104:107], v245 offset:50176\n"
"  ds_read_b128 v[108:111], v245 offset:51200\n"
"  ds_read_b128 v[112:115], v245 offset:52224\n"
"  s_add_u32 m0, s80, 16384\n"
"  buffer_load_dword v134, v239, s[20:23], s78 offen sc1\n"
"  buffer_load_dwordx4 v254, s[20:23], s78 offen sc1 lds\n"
"  buffer_load_dwordx4 v254, s[20:23], s78 offen offset:1024 sc1 lds\n"
"  buffer_load_dwordx4 v254, s[20:23], s78 offen offset:2048 sc1 lds\n"
"  buffer_load_dwordx4 v254, s[20:23], s78 offen offset:3072 sc1 lds\n"
"  s_cmp_lt_u32 s48, 1\n"
"  s_cbranch_scc1 .Lfb_L_kvk0\n"
".Lfb_kvc0:\n"
"  s_waitcnt lgkmcnt(3)\n"
"  v_add3_u32 v243, v100, v101, s56\n"
"  v_add3_u32 v243, v102, v103, v243\n"
"  s_waitcnt lgkmcnt(2)\n"
"  v_add3_u32 v243, v104, v105, v243\n"
"  v_add3_u32 v243, v106, v107, v243\n"
"  s_waitcnt lgkmcnt(1)\n"
"  v_add3_u32 v243, v108, v109, v243\n"
"  v_add3_u32 v243, v110, v111, v243\n"
"  s_waitcnt lgkmcnt(0)\n"
"  v_add3_u32 v243, v112, v113, v243\n"
"  v_add3_u32 v243, v114, v115, v243\n"
"  v_cmp_ne_u32 vcc, v243, v132\n"
"  s_cbranch_vccnz .Lfb_kvr0\n"
".Lfb_L_kvk0:\n"
"  s_barrier\n"
"  s_cmp_lt_u32 s48, 1\n"
"  s_cbranch_scc1 .Lfb_L_p7\n"
"  ds_read_b128 v[80:83], v200 offset:57344\n"
"  v_sub_u32 v201, v201, v202\n"
"  ds_read_b128 v[84:87], v201\n"
"  ds_read_b128 v[88:91], v201 offset:1024\n"
"  s_waitcnt lgkmcnt(2)\n"
"  v_mfma_f32_32x32x16_bf16 v[32:47], v[80:83], v[196:199], 0\n"
".Lfb_L_p7:\n"
"  s_add_u32 m0, s80, 0\n"
"  buffer_load_dword v135, v239, s[20:23], s79 offen sc1\n"
"  buffer_load_dwordx4 v254, s[20:23], s79 offen sc1 lds\n"
"  buffer_load_dwordx4 v254, s[20:23], s79 offen offset:1024 sc1 lds\n"
"  buffer_load_dwordx4 v254, s[20:23], s79 offen offset:2048 sc1 lds\n"
"  buffer_load_dwordx4 v254, s[20:23], s79 offen offset:3072 sc1 lds\n"
"  s_cmp_lt_u32 s48, 1\n"
"  s_cbranch_scc1 .Lfb_L_g7\n"
"  ds_read_b128 v[80:83], v200 offset:49152\n"
"  v_sub_u32 v201, v201, v202\n"
"  ds_read_b128 v[92:95], v201\n"
"  ds_read_b128 v[96:99], v201 offset:1024\n"
"  v_exp_f32 v32, v32\n"
"  v_exp_f32 v33, v33\n"
"  v_exp_f32 v34, v34\n"
"  v_exp_f32 v35, v35\n"
"  v_cvt_pk_bf16_f32 v32, v32, v33\n"
"  v_exp_f32 v36, v36\n"
"  v_exp_f32 v37, v37\n"
"  v_cvt_pk_bf16_f32 v33, v34, v35\n"
"  v_exp_f32 v38, v38\n"
"  v_exp_f32 v39, v39\n"
"  v_cvt_pk_bf16_f32 v34, v36, v37\n"
"  v_exp_f32 v40, v40\n"
"  v_cvt_pk_bf16_f32 v35, v38, v39\n"
"  v_exp_f32 v41, v41\n"
"  v_exp_f32 v42, v42\n"
"  s_waitcnt lgkmcnt(4)\n"
"  v_mfma_f32_32x32x16_bf16 v[64:79], v[84:87], v[32:35], v[64:79]\n"
"  v_exp_f32 v43, v43\n"
"  v_cvt_pk_bf16_f32 v40, v40, v41\n"
"  s_waitcnt lgkmcnt(2)\n"
"  v_mfma_f32_32x32x16_bf16 v[48:63], v[80:83], v[196:199], 0\n"
"  s_waitcnt vmcnt(10)\n"
"  ds_read_b128 v[116:119], v245 offset:32768\n"
"  ds_read_b128 v[120:123], v245 offset:33792\n"
"  ds_read_b128 v[124:127], v245 offset:34816\n"
"  ds_read_b128 v[128:131], v245 offset:35840\n"
"  v_exp_f32 v44, v44\n"
"  v_exp_f32 v45, v45\n"
"  v_cvt_pk_bf16_f32 v41, v42, v43\n"
"  v_exp_f32 v46, v46\n"
"  v_exp_f32 v47, v47\n"
"  v_cvt_pk_bf16_f32 v42, v44, v45\n"
"  v_cvt_pk_bf16_f32 v43, v46, v47\n"
"  s_nop 1\n"
"  v_mfma_f32_32x32x16_bf16 v[64:79], v[88:91], v[40:43], v[64:79]\n"
".Lfb_L_g7:\n"
"  s_cmp_lt_u32 s48, 2\n"
"  s_cbranch_scc1 .Lfb_L_kvk1\n"
".Lfb_kvc1:\n"
"  s_waitcnt lgkmcnt(3)\n"
"  v_add3_u32 v243, v116, v117, s56\n"
"  v_add3_u32 v243, v118, v119, v243\n"
"  s_waitcnt lgkmcnt(2)\n"
"  v_add3_u32 v243, v120, v121, v243\n"
"  v_add3_u32 v243, v122, v123, v243\n"
"  s_waitcnt lgkmcnt(1)\n"
"  v_add3_u32 v243, v124, v125, v243\n"
"  v_add3_u32 v243, v126, v127, v243\n"
"  s_waitcnt lgkmcnt(0)\n"
"  v_add3_u32 v243, v128, v129, v243\n"
"  v_add3_u32 v243, v130, v131, v243\n"
"  s_waitcnt vmcnt(14)\n"
"  v_cmp_ne_u32 vcc, v243, v133\n"
"  s_cbranch_vccnz .Lfb_kvr1\n"
".Lfb_L_kvk1:\n"
"  s_barrier\n"
"  s_cmp_lt_u32 s48, 1\n"
"  s_cbranch_scc1 .Lfb_L_g6\n"
"  ds_read_b128 v[80:83], v200 offset:40960\n"
"  v_sub_u32 v201, v201, v202\n"
"  ds_read_b128 v[84:87], v201\n"
"  ds_read_b128 v[88:91], v201 offset:1024\n"
"  v_exp_f32 v48, v48\n"
"  v_exp_f32 v49, v49\n"
"  v_exp_f32 v50, v50\n"
"  v_exp_f32 v51, v51\n"
"  v_cvt_pk_bf16_f32 v48, v48, v49\n"
"  v_exp_f32 v52, v52\n"
"  v_exp_f32 v53, v53\n"
"  v_cvt_pk_bf16_f32 v49, v50, v51\n"
"  v_exp_f32 v54, v54\n"
"  v_exp_f32 v55, v55\n"
"  v_cvt_pk_bf16_f32 v50, v52, v53\n"
"  v_exp_f32 v56, v56\n"
"  v_cvt_pk_bf16_f32 v51, v54, v55\n"
"  v_exp_f32 v57, v57\n"
"  v_exp_f32 v58, v58\n"
"  s_waitcnt lgkmcnt(8)\n"
"  v_mfma_f32_32x32x16_bf16 v[64:79], v[92:95], v[48:51], v[64:79]\n"
"  v_exp_f32 v59, v59\n"
"  v_cvt_pk_bf16_f32 v56, v56, v57\n"
"  s_waitcnt lgkmcnt(2)\n"
"  v_mfma_f32_32x32x16_bf16 v[32:47], v[80:83], v[196:199], 0\n"
"  s_waitcnt vmcnt(5)\n"
"  ds_read_b128 v[100:103], v245 offset:16384\n"
"  ds_read_b128 v[104:107], v245 offset:17408\n"
"  ds_read_b128 v[108:111], v245 offset:18432\n"
"  ds_read_b128 v[112:115], v245 offset:19456\n"
"  v_exp_f32 v60, v60\n"
"  v_exp_f32 v61, v61\n"
"  v_cvt_pk_bf16_f32 v57, v58, v59\n"
"  v_exp_f32 v62, v62\n"
"  v_exp_f32 v63, v63\n"
"  v_cvt_pk_bf16_f32 v58, v60, v61\n"
"  v_cvt_pk_bf16_f32 v59, v62, v63\n"
"  s_nop 1\n"
"  v_mfma_f32_32x32x16_bf16 v[64:79], v[96:99], v[56:59], v[64:79]\n"
".Lfb_L_g6:\n"
"  s_cmp_lt_u32 s48, 3\n"
"  s_cbranch_scc1 .Lfb_L_kvk2\n"
".Lfb_kvc2:\n"
"  s_waitcnt lgkmcnt(3)\n"
"  v_add3_u32 v243, v100, v101, s56\n"
"  v_add3_u32 v243, v102, v103, v243\n"
"  s_waitcnt lgkmcnt(2)\n"
"  v_add3_u32 v243, v104, v105, v243\n"
"  v_add3_u32 v243, v106, v107, v243\n"
"  s_waitcnt lgkmcnt(1)\n"
"  v_add3_u32 v243, v108, v109, v243\n"
"  v_add3_u32 v243, v110, v111, v243\n"
"  s_waitcnt lgkmcnt(0)\n"
"  v_add3_u32 v243, v112, v113, v243\n"
"  v_add3_u32 v243, v114, v115, v243\n"
"  s_waitcnt vmcnt(9)\n"
"  v_cmp_ne_u32 vcc, v243, v134\n"
"  s_cbranch_vccnz .Lfb_kvr2\n"
".Lfb_L_kvk2:\n"
"  s_cmp_lt_u32 s48, 2\n"
"  s_cbranch_scc1 .Lfb_L_g5\n"
"  ds_read_b128 v[80:83], v200 offset:32768\n"
"  v_sub_u32 v201, v201, v202\n"
"  ds_read_b128 v[92:95], v201\n"
"  ds_read_b128 v[96:99], v201 offset:1024\n"
"  v_exp_f32 v32, v32\n"
"  v_exp_f32 v33, v33\n"
"  v_exp_f32 v34, v34\n"
"  v_exp_f32 v35, v35\n"
"  v_cvt_pk_bf16_f32 v32, v32, v33\n"
"  v_exp_f32 v36, v36\n"
"  v_exp_f32 v37, v37\n"
"  v_cvt_pk_bf16_f32 v33, v34, v35\n"
"  v_exp_f32 v38, v38\n"
"  v_exp_f32 v39, v39\n"
"  v_cvt_pk_bf16_f32 v34, v36, v37\n"
"  v_exp_f32 v40, v40\n"
"  v_cvt_pk_bf16_f32 v35, v38, v39\n"
"  v_exp_f32 v41, v41\n"
"  v_exp_f32 v42, v42\n"
"  s_waitcnt lgkmcnt(4)\n"
"  v_mfma_f32_32x32x16_bf16 v[64:79], v[84:87], v[32:35], v[64:79]\n"
"  v_exp_f32 v43, v43\n"
"  v_cvt_pk_bf16_f32 v40, v40, v41\n"
"  s_waitcnt lgkmcnt(2)\n"
"  v_mfma_f32_32x32x16_bf16 v[48:63], v[80:83], v[196:199], 0\n"
"  s_waitcnt vmcnt(0)\n"
"  ds_read_b128 v[116:119], v245\n"
"  ds_read_b128 v[120:123], v245 offset:1024\n"
"  ds_read_b128 v[124:127], v245 offset:2048\n"
"  ds_read_b128 v[128:131], v245 offset:3072\n"
"  v_exp_f32 v44, v44\n"
"  v_exp_f32 v45, v45\n"
"  v_cvt_pk_bf16_f32 v41, v42, v43\n"
"  v_exp_f32 v46, v46\n"
"  v_exp_f32 v47, v47\n"
"  v_cvt_pk_bf16_f32 v42, v44, v45\n"
"  v_cvt_pk_bf16_f32 v43, v46, v47\n"
"  s_nop 1\n"
"  v_mfma_f32_32x32x16_bf16 v[64:79], v[88:91], v[40:43], v[64:79]\n"
".Lfb_L_g5:\n"
"  s_cmp_lt_u32 s48, 4\n"
"  s_cbranch_scc1 .Lfb_L_kvk3\n"
".Lfb_kvc3:\n"
"  s_waitcnt lgkmcnt(3)\n"
"  v_add3_u32 v243, v116, v117, s56\n"
"  v_add3_u32 v243, v118, v119, v243\n"
"  s_waitcnt lgkmcnt(2)\n"
"  v_add3_u32 v243, v120, v121, v243\n"
"  v_add3_u32 v243, v122, v123, v243\n"
"  s_waitcnt lgkmcnt(1)\n"
"  v_add3_u32 v243, v124, v125, v243\n"
"  v_add3_u32 v243, v126, v127, v243\n"
"  s_waitcnt lgkmcnt(0)\n"
"  v_add3_u32 v243, v128, v129, v243\n"
"  v_add3_u32 v243, v130, v131, v243\n"
"  s_waitcnt vmcnt(4)\n"
"  v_cmp_ne_u32 vcc, v243, v135\n"
"  s_cbranch_vccnz .Lfb_kvr3\n"
".Lfb_L_kvk3:\n"
"  s_waitcnt vmcnt(0)\n"
"  s_barrier\n"
"  s_cmp_lt_u32 s48, 2\n"
"  s_cbranch_scc1 .Lfb_L_g4\n"
"  ds_read_b128 v[80:83], v200 offset:24576\n"
"  v_sub_u32 v201, v201, v202\n"
"  ds_read_b128 v[84:87], v201\n"
"  ds_read_b128 v[88:91], v201 offset:1024\n"
"  v_exp_f32 v48, v48\n"
"  v_exp_f32 v49, v49\n"
"  v_exp_f32 v50, v50\n"
"  v_exp_f32 v51, v51\n"
"  v_cvt_pk_bf16_f32 v48, v48, v49\n"
"  v_exp_f32 v52, v52\n"
"  v_exp_f32 v53, v53\n"
"  v_cvt_pk_bf16_f32 v49, v50, v51\n"
"  v_exp_f32 v54, v54\n"
"  v_exp_f32 v55, v55\n"
"  v_cvt_pk_bf16_f32 v50, v52, v53\n"
"  v_exp_f32 v56, v56\n"
"  v_cvt_pk_bf16_f32 v51, v54, v55\n"
"  v_exp_f32 v57, v57\n"
"  v_exp_f32 v58, v58\n"
"  s_waitcnt lgkmcnt(8)\n"
"  v_mfma_f32_32x32x16_bf16 v[64:79], v[92:95], v[48:51], v[64:79]\n"
"  v_exp_f32 v59, v59\n"
"  v_cvt_pk_bf16_f32 v56, v56, v57\n"
"  s_waitcnt lgkmcnt(2)\n"
"  v_mfma_f32_32x32x16_bf16 v[32:47], v[80:83], v[196:199], 0\n"
"  v_exp_f32 v60, v60\n"
"  v_exp_f32 v61, v61\n"
"  v_cvt_pk_bf16_f32 v57, v58, v59\n"
"  v_exp_f32 v62, v62\n"
"  v_exp_f32 v63, v63\n"
"  v_cvt_pk_bf16_f32 v58, v60, v61\n"
"  v_cvt_pk_bf16_f32 v59, v62, v63\n"
"  s_nop 1\n"
"  v_mfma_f32_32x32x16_bf16 v[64:79], v[96:99], v[56:59], v[64:79]\n"
".Lfb_L_g4:\n"
"  buffer_load_dwordx4 v[212:215], v247, s[4:7], s73 offen\n"
"  buffer_load_dwordx4 v[216:219], v247, s[32:35], s74 offen\n"
"  s_cmp_lt_u32 s48, 3\n"
"  s_cbranch_scc1 .Lfb_L_g2\n"
"  ds_read_b128 v[80:83], v200 offset:16384\n"
"  v_sub_u32 v201, v201, v202\n"
"  ds_read_b128 v[92:95], v201\n"
"  ds_read_b128 v[96:99], v201 offset:1024\n"
"  v_exp_f32 v32, v32\n"
"  v_exp_f32 v33, v33\n"
"  v_exp_f32 v34, v34\n"
"  v_exp_f32 v35, v35\n"
"  v_cvt_pk_bf16_f32 v32, v32, v33\n"
"  v_exp_f32 v36, v36\n"
"  v_exp_f32 v37, v37\n"
"  v_cvt_pk_bf16_f32 v33, v34, v35\n"
"  v_exp_f32 v38, v38\n"
"  v_exp_f32 v39, v39\n"
"  v_cvt_pk_bf16_f32 v34, v36, v37\n"
"  v_exp_f32 v40, v40\n"
"  v_cvt_pk_bf16_f32 v35, v38, v39\n"
"  v_exp_f32 v41, v41\n"
"  v_exp_f32 v42, v42\n"
"  s_waitcnt lgkmcnt(4)\n"
"  v_mfma_f32_32x32x16_bf16 v[64:79], v[84:87], v[32:35], v[64:79]\n"
"  v_exp_f32 v43, v43\n"
"  v_cvt_pk_bf16_f32 v40, v40, v41\n"
"  s_waitcnt lgkmcnt(2)\n"
"  v_mfma_f32_32x32x16_bf16 v[48:63], v[80:83], v[196:199], 0\n"
"  v_exp_f32 v44, v44\n"
"  v_exp_f32 v45, v45\n"
"  v_cvt_pk_bf16_f32 v41, v42, v43\n"
"  v_exp_f32 v46, v46\n"
"  v_exp_f32 v47, v47\n"
"  v_cvt_pk_bf16_f32 v42, v44, v45\n"
"  v_cvt_pk_bf16_f32 v43, v46, v47\n"
"  s_nop 1\n"
"  v_mfma_f32_32x32x16_bf16 v[64:79], v[88:91], v[40:43], v[64:79]\n"
"  ds_read_b128 v[80:83], v200 offset:8192\n"
"  v_sub_u32 v201, v201, v202\n"
"  ds_read_b128 v[84:87], v201\n"
"  ds_read_b128 v[88:91], v201 offset:1024\n"
"  v_exp_f32 v48, v48\n"
"  v_exp_f32 v49, v49\n"
"  v_exp_f32 v50, v50\n"
"  v_exp_f32 v51, v51\n"
"  v_cvt_pk_bf16_f32 v48, v48, v49\n"
"  v_exp_f32 v52, v52\n"
"  v_exp_f32 v53, v53\n"
"  v_cvt_pk_bf16_f32 v49, v50, v51\n"
"  v_exp_f32 v54, v54\n"
"  v_exp_f32 v55, v55\n"
"  v_cvt_pk_bf16_f32 v50, v52, v53\n"
"  v_exp_f32 v56, v56\n"
"  v_cvt_pk_bf16_f32 v51, v54, v55\n"
"  v_exp_f32 v57, v57\n"
"  v_exp_f32 v58, v58\n"
"  s_waitcnt lgkmcnt(4)\n"
"  v_mfma_f32_32x32x16_bf16 v[64:79], v[92:95], v[48:51], v[64:79]\n"
"  v_exp_f32 v59, v59\n"
"  v_cvt_pk_bf16_f32 v56, v56, v57\n"
"  s_cmp_eq_u32 s54, 0\n"
"  s_cbranch_scc1 .Lfb_L_s2a\n"
"  s_waitcnt lgkmcnt(2)\n"
"  v_mfma_f32_32x32x16_bf16 v[32:47], v[80:83], v[196:199], v[180:195]\n"
"  s_branch .Lfb_L_s2b\n"
".Lfb_L_s2a:\n"
"  s_waitcnt lgkmcnt(2)\n"
"  v_mfma_f32_32x32x16_bf16 v[32:47], v[80:83], v[196:199], 0\n"
".Lfb_L_s2b:\n"
"  v_exp_f32 v60, v60\n"
"  v_exp_f32 v61, v61\n"
"  v_cvt_pk_bf16_f32 v57, v58, v59\n"
"  v_exp_f32 v62, v62\n"
"  v_exp_f32 v63, v63\n"
"  v_cvt_pk_bf16_f32 v58, v60, v61\n"
"  v_cvt_pk_bf16_f32 v59, v62, v63\n"
"  s_nop 1\n"
"  v_mfma_f32_32x32x16_bf16 v[64:79], v[96:99], v[56:59], v[64:79]\n"
".Lfb_L_g2:\n"
"  s_cmp_lt_u32 s48, 4\n"
"  s_cbranch_scc1 .Lfb_L_g1\n"
"  ds_read_b128 v[80:83], v200\n"
"  v_sub_u32 v201, v201, v202\n"
"  ds_read_b128 v[92:95], v201\n"
"  ds_read_b128 v[96:99], v201 offset:1024\n"
"  v_exp_f32 v32, v32\n"
"  v_exp_f32 v33, v33\n"
"  v_exp_f32 v34, v34\n"
"  v_exp_f32 v35, v35\n"
"  v_cvt_pk_bf16_f32 v32, v32, v33\n"
"  v_exp_f32 v36, v36\n"
"  v_exp_f32 v37, v37\n"
"  v_cvt_pk_bf16_f32 v33, v34, v35\n"
"  v_exp_f32 v38, v38\n"
"  v_exp_f32 v39, v39\n"
"  v_cvt_pk_bf16_f32 v34, v36, v37\n"
"  v_exp_f32 v40, v40\n"
"  v_cvt_pk_bf16_f32 v35, v38, v39\n"
"  v_exp_f32 v41, v41\n"
"  v_exp_f32 v42, v42\n"
"  s_waitcnt lgkmcnt(4)\n"
"  v_mfma_f32_32x32x16_bf16 v[64:79], v[84:87], v[32:35], v[64:79]\n"
"  v_exp_f32 v43, v43\n"
"  v_cvt_pk_bf16_f32 v40, v40, v41\n"
"  s_waitcnt lgkmcnt(2)\n"
"  v_mfma_f32_32x32x16_bf16 v[48:63], v[80:83], v[196:199], v[180:195]\n"
"  v_exp_f32 v44, v44\n"
"  v_exp_f32 v45, v45\n"
"  v_cvt_pk_bf16_f32 v41, v42, v43\n"
"  v_exp_f32 v46, v46\n"
"  v_exp_f32 v47, v47\n"
"  v_cvt_pk_bf16_f32 v42, v44, v45\n"
"  v_cvt_pk_bf16_f32 v43, v46, v47\n"
"  s_nop 1\n"
"  v_mfma_f32_32x32x16_bf16 v[64:79], v[88:91], v[40:43], v[64:79]\n"
"  s_cmp_eq_u32 s54, 1\n"
"  s_cbranch_scc1 .Lfb_L_g1\n"
"  v_exp_f32 v48, v48\n"
"  v_exp_f32 v49, v49\n"
"  v_exp_f32 v50, v50\n"
"  v_exp_f32 v51, v51\n"
"  v_cvt_pk_bf16_f32 v48, v48, v49\n"
"  v_exp_f32 v52, v52\n"
"  v_exp_f32 v53, v53\n"
"  v_cvt_pk_bf16_f32 v49, v50, v51\n"
"  v_exp_f32 v54, v54\n"
"  v_exp_f32 v55, v55\n"
"  v_cvt_pk_bf16_f32 v50, v52, v53\n"
"  v_exp_f32 v56, v56\n"
"  v_cvt_pk_bf16_f32 v51, v54, v55\n"
"  v_exp_f32 v57, v57\n"
"  v_exp_f32 v58, v58\n"
"  s_waitcnt lgkmcnt(1)\n"
"  v_mfma_f32_32x32x16_bf16 v[64:79], v[92:95], v[48:51], v[64:79]\n"
"  v_exp_f32 v59, v59\n"
"  v_cvt_pk_bf16_f32 v56, v56, v57\n"
"  v_exp_f32 v60, v60\n"
"  v_exp_f32 v61, v61\n"
"  v_cvt_pk_bf16_f32 v57, v58, v59\n"
"  v_exp_f32 v62, v62\n"
"  v_exp_f32 v63, v63\n"
"  v_cvt_pk_bf16_f32 v58, v60, v61\n"
"  v_cvt_pk_bf16_f32 v59, v62, v63\n"
"  s_waitcnt lgkmcnt(0)\n"
"  s_nop 0\n"
"  v_mfma_f32_32x32x16_bf16 v[64:79], v[96:99], v[56:59], v[64:79]\n"
".Lfb_L_g1:\n"
"  s_lshl1_add_u32 s60, s48, s54\n"
"  s_lshl3_add_u32 s60, s60, s49\n"
"  s_lshl1_add_u32 s60, s60, s55\n"
"  s_mul_i32 s60, s60, 2560\n"
"  s_add_u32 s60, s60, 6292736\n"
"  s_mov_b32 s61, 0x21800000\n"
"  s_mov_b32 s62, 0x5d800000\n"
"  s_nop 4\n"
"  v_cmp_nlt_f32 vcc, s61, v72\n"
"  v_cmp_ngt_f32 s[90:91], s62, v72\n"
"  s_nop 0\n"
"  s_or_b64 vcc, vcc, s[90:91]\n"
"  s_cbranch_vccnz .Lfb_slow\n"
".Lfb_L_norm:\n"
"  s_nop 3\n"
"  v_rcp_f32 v72, v72\n"
"  s_nop 0\n"
"  v_pk_mul_f32 v[64:65], v[64:65], v[72:73] op_sel_hi:[1,0]\n"
"  v_pk_mul_f32 v[66:67], v[66:67], v[72:73] op_sel_hi:[1,0]\n"
"  v_pk_mul_f32 v[68:69], v[68:69], v[72:73] op_sel_hi:[1,0]\n"
"  v_pk_mul_f32 v[70:71], v[70:71], v[72:73] op_sel_hi:[1,0]\n"
"  v_cvt_pk_bf16_f32 v64, v64, v65\n"
"  v_cvt_pk_bf16_f32 v65, v66, v67\n"
"  v_cvt_pk_bf16_f32 v66, v68, v69\n"
"  v_cvt_pk_bf16_f32 v67, v70, v71\n"
"  buffer_store_dwordx4 v[64:67], v254, s[20:23], s60 offen sc1\n"
"  v_add3_u32 v73, v64, v65, s56\n"
"  v_add3_u32 v73, v73, v66, v67\n"
"  buffer_store_dword v73, v253, s[20:23], s60 offen offset:1024 sc1\n"
"  s_waitcnt lgkmcnt(0)\n"
"  s_barrier\n"
"  s_add_u32 s60, s89, 5\n"
"  s_and_b32 s60, s60, 7\n"
"  s_lshl_b32 s60, s60, 16\n"
"  s_lshl_b32 s61, s45, 10\n"
"  s_add_u32 s60, s60, s88\n"
"  s_add_u32 m0, s61, 106496\n"
"  s_nop 0\n"
"  buffer_load_dwordx4 v207, s[16:19], s60 offen lds\n"
"  s_add_u32 m0, s61, 110592\n"
"  s_add_u32 s60, s60, 16384\n"
"  buffer_load_dwordx4 v207, s[16:19], s60 offen lds\n"
"  s_add_u32 m0, s61, 114688\n"
"  s_add_u32 s60, s60, 16384\n"
"  buffer_load_dwordx4 v207, s[16:19], s60 offen lds\n"
"  s_add_u32 m0, s61, 118784\n"
"  s_add_u32 s60, s60, 16384\n"
"  buffer_load_dwordx4 v207, s[16:19], s60 offen lds\n"
"  s_add_u32 s60, s89, 6\n"
"  s_and_b32 s60, s60, 7\n"
"  s_lshl_b32 s60, s60, 16\n"
"  s_lshl_b32 s61, s45, 10\n"
"  s_add_u32 s60, s60, s88\n"
"  s_add_u32 m0, s61, 122880\n"
"  s_nop 0\n"
"  buffer_load_dwordx4 v207, s[16:19], s60 offen lds\n"
"  s_add_u32 m0, s61, 126976\n"
"  s_add_u32 s60, s60, 16384\n"
"  buffer_load_dwordx4 v207, s[16:19], s60 offen lds\n"
"  s_add_u32 m0, s61, 131072\n"
"  s_add_u32 s60, s60, 16384\n"
"  buffer_load_dwordx4 v207, s[16:19], s60 offen lds\n"
"  s_add_u32 m0, s61, 135168\n"
"  s_add_u32 s60, s60, 16384\n"
"  buffer_load_dwordx4 v207, s[16:19], s60 offen lds\n"
"  s_add_u32 s60, s89, 7\n"
"  s_and_b32 s60, s60, 7\n"
"  s_lshl_b32 s60, s60, 16\n"
"  s_lshl_b32 s61, s45, 10\n"
"  s_add_u32 s60, s60, s88\n"
"  s_add_u32 m0, s61, 139264\n"
"  s_nop 0\n"
"  buffer_load_dwordx4 v207, s[16:19], s60 offen lds\n"
"  s_add_u32 m0, s61, 143360\n"
"  s_add_u32 s60, s60, 16384\n"
"  buffer_load_dwordx4 v207, s[16:19], s60 offen lds\n"
"  s_add_u32 m0, s61, 147456\n"
"  s_add_u32 s60, s60, 16384\n"
"  buffer_load_dwordx4 v207, s[16:19], s60 offen lds\n"
"  s_add_u32 m0, s61, 151552\n"
"  s_add_u32 s60, s60, 16384\n"
"  buffer_load_dwordx4 v207, s[16:19], s60 offen lds\n"
"  v_add_u32 v245, 256, v254\n"
"  s_sleep 4\n"
"  s_add_u32 m0, s84, 0\n"
"  buffer_load_dword v32, v253, s[20:23], s68 offen offset:1024 sc1\n"
"  buffer_load_dword v33, v253, s[20:23], s68 offen offset:2304 sc1\n"
"  buffer_load_dwordx4 v254, s[20:23], s68 offen sc1 lds\n"
"  buffer_load_dwordx4 v245, s[20:23], s68 offen offset:1024 sc1 lds\n"
"  s_add_u32 m0, s84, 8192\n"
"  buffer_load_dword v34, v253, s[20:23], s69 offen offset:1024 sc1\n"
"  buffer_load_dword v35, v253, s[20:23], s69 offen offset:2304 sc1\n"
"  buffer_load_dwordx4 v254, s[20:23], s69 offen sc1 lds\n"
"  buffer_load_dwordx4 v245, s[20:23], s69 offen offset:1024 sc1 lds\n"
"  s_add_u32 m0, s84, 16384\n"
"  buffer_load_dword v36, v253, s[20:23], s70 offen offset:1024 sc1\n"
"  buffer_load_dword v37, v253, s[20:23], s70 offen offset:2304 sc1\n"
"  buffer_load_dwordx4 v254, s[20:23], s70 offen sc1 lds\n"
"  buffer_load_dwordx4 v245, s[20:23], s70 offen offset:1024 sc1 lds\n"
"  s_add_u32 m0, s84, 24576\n"
"  buffer_load_dword v38, v253, s[20:23], s71 offen offset:1024 sc1\n"
"  buffer_load_dword v39, v253, s[20:23], s71 offen offset:2304 sc1\n"
"  buffer_load_dwordx4 v254, s[20:23], s71 offen sc1 lds\n"
"  buffer_load_dwordx4 v245, s[20:23], s71 offen offset:1024 sc1 lds\n"
"  v_add_u32 v44, 158720, v254\n"
"  ds_read_b128 v[44:47], v44\n"
"  s_waitcnt lgkmcnt(0)\n"
"  v_lshlrev_b32 v208, 16, v44\n"
"  v_and_b32 v48, s59, v44\n"
"  v_add_f32 v208, v208, v48\n"
"  v_lshlrev_b32 v48, 16, v45\n"
"  v_add_f32 v208, v208, v48\n"
"  v_and_b32 v48, s59, v45\n"
"  v_add_f32 v208, v208, v48\n"
"  v_lshlrev_b32 v48, 16, v46\n"
"  v_add_f32 v208, v208, v48\n"
"  v_and_b32 v48, s59, v46\n"
"  v_add_f32 v208, v208, v48\n"
"  v_lshlrev_b32 v48, 16, v47\n"
"  v_add_f32 v208, v208, v48\n"
"  v_and_b32 v48, s59, v47\n"
"  v_add_f32 v208, v208, v48\n"
"  s_nop 1\n"
"  v_add_f32_dpp v208, v208, v208 quad_perm:[1,0,3,2] row_mask:0xf bank_mask:0xf\n"
"  s_nop 1\n"
"  v_add_f32_dpp v208, v208, v208 quad_perm:[2,3,0,1] row_mask:0xf bank_mask:0xf\n"
"  s_nop 1\n"
"  v_add_f32_dpp v208, v208, v208 row_half_mirror row_mask:0xf bank_mask:0xf\n"
"  s_nop 1\n"
"  v_add_f32_dpp v208, v208, v208 row_mirror row_mask:0xf bank_mask:0xf\n"
"  v_mov_b32 v44, v208\n"
"  s_nop 1\n"
"  v_permlane16_swap_b32 v208, v44\n"
"  v_add_f32 v208, v208, v44\n"
"  v_mov_b32 v44, v208\n"
"  s_nop 1\n"
"  v_permlane32_swap_b32 v208, v44\n"
"  v_add_f32 v208, v208, v44\n"
"  s_add_u32 s60, s50, s44\n"
"  s_and_b32 s60, s60, 7\n"
"  s_lshl3_add_u32 s60, s46, s60\n"
"  s_lshl_b32 s80, s60, 11\n"
"  s_add_u32 s80, s80, 65536\n"
"  s_lshl_b32 s60, s60, 5\n"
"  s_sub_u32 s60, 2047, s60\n"
"  v_and_b32 v227, 31, v255\n"
"  v_sub_u32 v227, s60, v227\n"
"  s_lshl2_add_u32 s60, s44, 155904\n"
"  v_mov_b32 v228, s60\n"
"  s_sub_u32 s60, s46, 1\n"
"  s_lshl_b32 s60, s60, 5\n"
"  s_add_u32 s60, s60, s47\n"
"  s_lshr_b32 s60, s60, 1\n"
"  s_mov_b32 s81, 0x3b000000\n"
"  s_mov_b32 s82, 0xfffff800\n"
"  s_lshl_b32 s76, s45, 13\n"
"  s_lshl_b32 s60, s60, 5\n"
"  s_mul_i32 s77, s45, 458752\n"
"  s_add_u32 s77, s77, s60\n"
"  v_lshrrev_b32 v243, 4, v255\n"
"  v_bfe_u32 v244, v255, 1, 3\n"
"  v_lshl_or_b32 v246, v243, 5, v244\n"
"  v_mul_u32_u24 v246, 3584, v246\n"
"  v_and_b32 v244, 16, v254\n"
"  v_add_u32 v246, v246, v244\n"
"  v_lshl_add_u32 v248, v243, 8, s76\n"
"  v_bfe_u32 v244, v255, 2, 2\n"
"  v_lshl_add_u32 v248, v244, 5, v248\n"
"  v_and_b32 v244, 3, v255\n"
"  v_lshl_add_u32 v248, v244, 3, v248\n"
"  v_and_b32 v244, 15, v255\n"
"  s_lshl3_add_u32 s60, s45, 157696\n"
"  v_lshl_add_u32 v249, v244, 5, s60\n"
".Lfb_oc0:\n"
"  s_waitcnt vmcnt(12)\n"
"  ds_read_b128 v[0:3], v236\n"
"  ds_read_b128 v[4:7], v236 offset:1024\n"
"  s_waitcnt lgkmcnt(1)\n"
"  v_add3_u32 v243, v0, v1, s56\n"
"  v_add3_u32 v243, v243, v2, v3\n"
"  v_cmp_ne_u32 vcc, v243, v32\n"
"  s_waitcnt lgkmcnt(0)\n"
"  v_add3_u32 v244, v4, v5, s56\n"
"  v_add3_u32 v244, v244, v6, v7\n"
"  v_cmp_ne_u32 s[90:91], v244, v33\n"
"  s_nop 0\n"
"  s_or_b64 vcc, vcc, s[90:91]\n"
"  s_cbranch_vccnz .Lfb_or0\n"
".Lfb_ok0:\n"
"  s_barrier\n"
".Lfb_oc1:\n"
"  s_waitcnt vmcnt(8)\n"
"  ds_read_b128 v[0:3], v236 offset:8192\n"
"  ds_read_b128 v[4:7], v236 offset:9216\n"
"  s_waitcnt lgkmcnt(1)\n"
"  v_add3_u32 v243, v0, v1, s56\n"
"  v_add3_u32 v243, v243, v2, v3\n"
"  v_cmp_ne_u32 vcc, v243, v34\n"
"  s_waitcnt lgkmcnt(0)\n"
"  v_add3_u32 v244, v4, v5, s56\n"
"  v_add3_u32 v244, v244, v6, v7\n"
"  v_cmp_ne_u32 s[90:91], v244, v35\n"
"  s_nop 0\n"
"  s_or_b64 vcc, vcc, s[90:91]\n"
"  s_cbranch_vccnz .Lfb_or1\n"
".Lfb_ok1:\n"
"  s_barrier\n"
".Lfb_oc2:\n"
"  s_waitcnt vmcnt(4)\n"
"  ds_read_b128 v[0:3], v236 offset:16384\n"
"  ds_read_b128 v[4:7], v236 offset:17408\n"
"  s_waitcnt lgkmcnt(1)\n"
"  v_add3_u32 v243, v0, v1, s56\n"
"  v_add3_u32 v243, v243, v2, v3\n"
"  v_cmp_ne_u32 vcc, v243, v36\n"
"  s_waitcnt lgkmcnt(0)\n"
"  v_add3_u32 v244, v4, v5, s56\n"
"  v_add3_u32 v244, v244, v6, v7\n"
"  v_cmp_ne_u32 s[90:91], v244, v37\n"
"  s_nop 0\n"
"  s_or_b64 vcc, vcc, s[90:91]\n"
"  s_cbranch_vccnz .Lfb_or2\n"
".Lfb_ok2:\n"
"  s_barrier\n"
".Lfb_oc3:\n"
"  s_waitcnt vmcnt(0)\n"
"  ds_read_b128 v[0:3], v236 offset:24576\n"
"  ds_read_b128 v[4:7], v236 offset:25600\n"
"  s_waitcnt lgkmcnt(1)\n"
"  v_add3_u32 v243, v0, v1, s56\n"
"  v_add3_u32 v243, v243, v2, v3\n"
"  v_cmp_ne_u32 vcc, v243, v38\n"
"  s_waitcnt lgkmcnt(0)\n"
"  v_add3_u32 v244, v4, v5, s56\n"
"  v_add3_u32 v244, v244, v6, v7\n"
"  v_cmp_ne_u32 s[90:91], v244, v39\n"
"  s_nop 0\n"
"  s_or_b64 vcc, vcc, s[90:91]\n"
"  s_cbranch_vccnz .Lfb_or3\n"
".Lfb_ok3:\n"
"  s_cmp_eq_u32 s3, 0\n"
"  s_cbranch_scc1 .Lfb_L_fe0\n"
"  s_add_u32 m0, s76, 0\n"
"  s_add_u32 s60, s77, 0\n"
"  buffer_load_dwordx4 v246, s[24:27], s60 offen lds\n"
"  s_add_u32 m0, s76, 4096\n"
"  s_nop 0\n"
"  buffer_load_dwordx4 v246, s[28:31], s60 offen lds\n"
".Lfb_L_fe0:\n"
"  s_barrier\n"
"  s_mul_i32 s84, s46, 12288\n"
"  s_mul_i32 s60, s45, 384\n"
"  s_add_u32 s84, s84, s60\n"
"  s_add_u32 s84, s84, 196608\n"
"  s_add_u32 s85, s84, 6144\n"
"  v_lshrrev_b32 v178, 4, v255\n"
"  v_and_b32 v177, 3, v255\n"
"  v_lshl_or_b32 v178, v178, 2, v177\n"
"  v_bfe_u32 v177, v255, 2, 2\n"
"  v_mul_u32_u24 v177, 1536, v177\n"
"  v_lshl_add_u32 v176, v178, 4, v177\n"
"  v_lshl_add_u32 v177, v178, 3, v177\n"
"  s_lshl_b32 s60, s44, 11\n"
"  s_add_u32 s60, s60, 32768\n"
"  v_add_u32 v243, s60, v254\n"
"  ds_write_b128 v243, v[212:215]\n"
"  ds_write_b128 v243, v[216:219] offset:1024\n"
"  v_lshl_add_u32 v243, v255, 1, s60\n"
"  v_mov_b64 v[96:97], 0\n"
"  v_mov_b64 v[98:99], 0\n"
"  v_mov_b64 v[100:101], 0\n"
"  v_mov_b64 v[102:103], 0\n"
"  v_mov_b64 v[104:105], 0\n"
"  v_mov_b64 v[106:107], 0\n"
"  v_mov_b64 v[108:109], 0\n"
"  v_mov_b64 v[110:111], 0\n"
"  ds_read_u16_d16_hi v96, v243 offset:0\n"
"  ds_read_u16_d16_hi v97, v243 offset:128\n"
"  ds_read_u16_d16_hi v98, v243 offset:256\n"
"  ds_read_u16_d16_hi v99, v243 offset:384\n"
"  ds_read_u16_d16_hi v100, v243 offset:512\n"
"  ds_read_u16_d16_hi v101, v243 offset:640\n"
"  ds_read_u16_d16_hi v102, v243 offset:768\n"
"  ds_read_u16_d16_hi v103, v243 offset:896\n"
"  ds_read_u16_d16_hi v104, v243 offset:1024\n"
"  ds_read_u16_d16_hi v105, v243 offset:1152\n"
"  ds_read_u16_d16_hi v106, v243 offset:1280\n"
"  ds_read_u16_d16_hi v107, v243 offset:1408\n"
"  ds_read_u16_d16_hi v108, v243 offset:1536\n"
"  ds_read_u16_d16_hi v109, v243 offset:1664\n"
"  ds_read_u16_d16_hi v110, v243 offset:1792\n"
"  ds_read_u16_d16_hi v111, v243 offset:1920\n"
"  v_add_u32 v243, 65536, v254\n"
"  s_lshl2_add_u32 s62, s52, s51\n"
"  s_lshl_b32 s62, s62, 9\n"
"  s_add_u32 s62, s62, 65536\n"
"  s_cmp_lg_u32 s44, 4\n"
"  s_barrier\n"
"  s_cbranch_scc1 .Lfb_L_nored\n"
"  ds_read_b96 v[0:2], v243\n"
"  ds_read_b96 v[4:6], v243 offset:1024\n"
"  ds_read_b96 v[8:10], v243 offset:2048\n"
"  ds_read_b96 v[12:14], v243 offset:3072\n"
"  s_waitcnt lgkmcnt(2)\n"
"  v_pk_add_f32 v[0:1], v[0:1], v[4:5]\n"
"  v_add_f32 v2, v2, v6\n"
"  s_waitcnt lgkmcnt(0)\n"
"  v_pk_add_f32 v[8:9], v[8:9], v[12:13]\n"
"  v_add_f32 v10, v10, v14\n"
"  v_pk_add_f32 v[0:1], v[0:1], v[8:9]\n"
"  v_add_f32 v2, v2, v10\n"
"  v_mov_b32 v4, v0\n"
"  v_mov_b32 v5, v1\n"
"  v_mov_b32 v6, v2\n"
"  v_permlane32_swap_b32 v0, v4\n"
"  v_permlane32_swap_b32 v1, v5\n"
"  v_permlane32_swap_b32 v2, v6\n"
"  v_pk_add_f32 v[0:1], v[0:1], v[4:5]\n"
"  v_add_f32 v2, v2, v6\n"
"  s_mov_b32 exec_hi, 0\n"
"  v_add3_u32 v3, v0, v1, s56\n"
"  v_add_u32 v3, v3, v2\n"
"  buffer_store_dwordx4 v[0:3], v254, s[20:23], s62 offen sc1\n"
"  s_mov_b64 exec, -1\n"
".Lfb_L_nored:\n"
"  s_barrier\n"
"  s_cmp_eq_u32 s3, 0\n"
"  s_cbranch_scc1 .Lfb_L_fe1\n"
"  s_add_u32 m0, s76, 1024\n"
"  s_add_u32 s60, s77, 28672\n"
"  buffer_load_dwordx4 v246, s[24:27], s60 offen lds\n"
"  s_add_u32 m0, s76, 5120\n"
"  s_nop 0\n"
"  buffer_load_dwordx4 v246, s[28:31], s60 offen lds\n"
".Lfb_L_fe1:\n"
"  s_sleep 2\n"
"  buffer_load_dwordx4 v[212:215], v254, s[20:23], s80 offen sc1\n"
"  buffer_load_dwordx4 v[216:219], v254, s[20:23], s80 offen offset:1024 sc1\n"
".Lfb_L_stc:\n"
"  s_waitcnt vmcnt(1)\n"
"  v_add3_u32 v220, v212, v213, s56\n"
"  s_waitcnt vmcnt(0)\n"
"  v_add3_u32 v221, v216, v217, s56\n"
"  v_add_f32 v212, v212, v216\n"
"  v_add_u32 v220, v220, v214\n"
"  v_add_u32 v221, v221, v218\n"
"  v_add_f32 v213, v213, v217\n"
"  v_mov_b32 v216, v212\n"
"  v_cmp_ne_u32 vcc, v220, v215\n"
"  v_cmp_ne_u32 s[90:91], v221, v219\n"
"  v_add_f32 v214, v214, v218\n"
"  v_mov_b32 v217, v213\n"
"  s_or_b64 vcc, vcc, s[90:91]\n"
"  v_permlane32_swap_b32 v212, v216\n"
"  v_mov_b32 v218, v214\n"
"  s_cbranch_vccnz .Lfb_L_str\n"
".Lfb_L_stk:\n"
"  v_permlane32_swap_b32 v213, v217\n"
"  v_add_f32 v224, v212, v216\n"
"  v_permlane32_swap_b32 v214, v218\n"
"  v_mul_f32 v224, 0x3b000000, v224\n"
"  v_add_f32 v213, v213, v217\n"
"  v_mul_f32 v220, v224, v224\n"
"  v_add_f32 v214, v214, v218\n"
"  v_fma_f32 v220, v213, s81, -v220\n"
"  v_fma_f32 v214, -v224, v208, v214\n"
"  v_max_f32 v220, 0, v220\n"
"  v_add_f32 v220, 0x3727c5ac, v220\n"
"  v_rsq_f32 v225, v220\n"
"  s_nop 0\n"
"  v_mul_f32 v226, v214, v225\n"
"  v_ashrrev_i32 v220, 31, v226\n"
"  v_or_b32 v220, 0x80000000, v220\n"
"  v_xor_b32 v226, v226, v220\n"
"  v_and_or_b32 v226, v226, s82, v227\n"
"  s_nop 1\n"
"  v_max_u32_dpp v226, v226, v226 quad_perm:[1,0,3,2] row_mask:0xf bank_mask:0xf\n"
"  s_nop 1\n"
"  v_max_u32_dpp v226, v226, v226 quad_perm:[2,3,0,1] row_mask:0xf bank_mask:0xf\n"
"  s_nop 1\n"
"  v_max_u32_dpp v226, v226, v226 row_half_mirror row_mask:0xf bank_mask:0xf\n"
"  s_nop 1\n"
"  v_max_u32_dpp v226, v226, v226 row_mirror row_mask:0xf bank_mask:0xf\n"
"  v_mov_b32 v220, v226\n"
"  s_nop 1\n"
"  v_permlane16_swap_b32 v226, v220\n"
"  v_max3_u32 v226, v226, v220, 1\n"
"  ds_write_b32 v228, v226\n"
"  s_cmp_eq_u32 s3, 0\n"
"  s_cbranch_scc1 .Lfb_L_np\n"
"  s_add_u32 m0, s76, 2048\n"
"  s_add_u32 s60, s77, 57344\n"
"  buffer_load_dwordx4 v246, s[24:27], s60 offen lds\n"
"  s_add_u32 m0, s76, 6144\n"
"  s_nop 0\n"
"  buffer_load_dwordx4 v246, s[28:31], s60 offen lds\n"
"  s_add_u32 m0, s76, 3072\n"
"  s_add_u32 s60, s77, 86016\n"
"  buffer_load_dwordx4 v246, s[24:27], s60 offen lds\n"
"  s_add_u32 m0, s76, 7168\n"
"  s_nop 0\n"
"  buffer_load_dwordx4 v246, s[28:31], s60 offen lds\n"
"  s_waitcnt lgkmcnt(0)\n"
"  s_barrier\n"
"  s_sub_u32 s60, s46, 1\n"
"  s_lshl_b32 s60, s60, 5\n"
"  s_add_u32 s60, s60, s47\n"
"  s_lshr_b32 s60, s60, 1\n"
"  s_mul_i32 s75, s44, 224\n"
"  s_add_u32 s75, s75, s60\n"
"  s_lshl_b32 s75, s75, 7\n"
"  s_add_u32 s75, s75, 655360\n"
"  v_lshrrev_b32 v112, 5, v255\n"
"  v_mul_u32_u24 v112, 14336, v112\n"
"  v_and_b32 v113, 0x7c, v253\n"
"  v_add_u32 v113, v113, v112\n"
"  v_and_b32 v112, 15, v255\n"
"  v_lshlrev_b32 v112, 5, v112\n"
"  v_add_u32 v112, 157696, v112\n"
"  v_and_b32 v114, 1, v255\n"
"  v_mul_lo_u32 v114, v114, s56\n"
"  s_mov_b32 s61, 0xbfb8aa3b\n"
"  v_mov_b32 v244, 155936\n"
"  s_waitcnt vmcnt(0)\n"
"  ds_read_b64_tr_b16 v[0:1], v248\n"
"  ds_read_b64_tr_b16 v[2:3], v248 offset:128\n"
"  ds_read_b64_tr_b16 v[4:5], v248 offset:1024\n"
"  ds_read_b64_tr_b16 v[6:7], v248 offset:1152\n"
"  ds_read_b64_tr_b16 v[8:9], v248 offset:2048\n"
"  ds_read_b64_tr_b16 v[10:11], v248 offset:2176\n"
"  ds_read_b64_tr_b16 v[12:13], v248 offset:3072\n"
"  ds_read_b64_tr_b16 v[14:15], v248 offset:3200\n"
"  ds_read_b64_tr_b16 v[16:17], v248 offset:4096\n"
"  ds_read_b64_tr_b16 v[18:19], v248 offset:4224\n"
"  ds_read_b64_tr_b16 v[20:21], v248 offset:5120\n"
"  ds_read_b64_tr_b16 v[22:23], v248 offset:5248\n"
"  ds_read_b64_tr_b16 v[24:25], v248 offset:6144\n"
"  ds_read_b64_tr_b16 v[26:27], v248 offset:6272\n"
"  ds_read_b64_tr_b16 v[28:29], v248 offset:7168\n"
"  ds_read_b64_tr_b16 v[30:31], v248 offset:7296\n"
"  s_sleep 8\n"
"  buffer_load_dwordx2 v[168:169], v177, s[20:23], s84 offen offset:256 sc1\n"
"  buffer_load_dwordx2 v[170:171], v177, s[20:23], s85 offen offset:256 sc1\n"
"  buffer_load_dwordx4 v[160:163], v176, s[20:23], s84 offen sc1\n"
"  buffer_load_dwordx4 v[164:167], v176, s[20:23], s85 offen sc1\n"
".Lfb_L_cac:\n"
"  s_waitcnt vmcnt(2)\n"
"  v_max_u32 v174, v168, v170\n"
"  v_add_u32 v172, s56, v168\n"
"  v_add_u32 v173, s56, v170\n"
"  v_max_u32_dpp v174, v174, v174 row_ror:4 row_mask:0xf bank_mask:0xf\n"
"  s_waitcnt vmcnt(1)\n"
"  v_add3_u32 v172, v172, v160, v161\n"
"  v_add3_u32 v172, v172, v162, v163\n"
"  v_max_u32_dpp v174, v174, v174 row_ror:8 row_mask:0xf bank_mask:0xf\n"
"  v_cmp_ne_u32 vcc, v172, v169\n"
"  v_readfirstlane_b32 s86, v174\n"
"  s_waitcnt vmcnt(0)\n"
"  v_add3_u32 v173, v173, v164, v165\n"
"  v_add3_u32 v173, v173, v166, v167\n"
"  v_cmp_ne_u32 s[90:91], v173, v171\n"
"  s_andn2_b32 s87, 2047, s86\n"
"  s_or_b64 vcc, vcc, s[90:91]\n"
"  s_lshr_b32 s87, s87, 8\n"
"  s_cbranch_vccnz .Lfb_L_car\n"
".Lfb_L_cak:\n"
"  s_cmp_lt_u32 s87, 4\n"
"  s_cbranch_scc1 .Lfb_L_cal\n"
"  v_mov_b64 v[160:161], v[164:165]\n"
"  v_mov_b64 v[162:163], v[166:167]\n"
".Lfb_L_cal:\n"
"  s_waitcnt lgkmcnt(14)\n"
"  s_nop 0\n"
"  v_mfma_f32_16x16x32_bf16 v[32:35], v[160:163], v[0:3], 0\n"
"  s_waitcnt lgkmcnt(12)\n"
"  v_mfma_f32_16x16x32_bf16 v[36:39], v[160:163], v[4:7], 0\n"
"  s_waitcnt lgkmcnt(10)\n"
"  v_mfma_f32_16x16x32_bf16 v[40:43], v[160:163], v[8:11], 0\n"
"  s_waitcnt lgkmcnt(8)\n"
"  v_mfma_f32_16x16x32_bf16 v[44:47], v[160:163], v[12:15], 0\n"
"  s_waitcnt lgkmcnt(6)\n"
"  v_mfma_f32_16x16x32_bf16 v[48:51], v[160:163], v[16:19], 0\n"
"  s_waitcnt lgkmcnt(4)\n"
"  v_mfma_f32_16x16x32_bf16 v[52:55], v[160:163], v[20:23], 0\n"
"  s_waitcnt lgkmcnt(2)\n"
"  v_mfma_f32_16x16x32_bf16 v[56:59], v[160:163], v[24:27], 0\n"
"  s_waitcnt lgkmcnt(0)\n"
"  v_mfma_f32_16x16x32_bf16 v[60:63], v[160:163], v[28:31], 0\n"
"  s_and_b32 s60, s87, 3\n"
"  s_lshl_b32 s60, s60, 4\n"
"  v_mov_b32 v243, s86\n"
"  s_lshl_b64 exec, 0xffff, s60\n"
"  ds_write_b32 v244, v243\n"
"  v_add_f32 v32, v32, v37\n"
"  v_add_f32 v42, v42, v47\n"
"  v_add_f32 v32, v32, v42\n"
"  v_add_f32 v33, v48, v53\n"
"  v_add_f32 v58, v58, v63\n"
"  v_add_f32 v33, v33, v58\n"
"  ds_write_b64 v249, v[32:33]\n"
"  s_mov_b64 exec, -1\n"
"  s_waitcnt lgkmcnt(0)\n"
"  s_barrier\n"
"  ds_read_b128 v[116:119], v112\n"
"  ds_read_b128 v[120:123], v112 offset:16\n"
"  s_waitcnt lgkmcnt(1)\n"
"  v_pk_add_f32 v[116:117], v[116:117], v[118:119]\n"
"  s_waitcnt lgkmcnt(0)\n"
"  v_pk_add_f32 v[120:121], v[120:121], v[122:123]\n"
"  v_pk_add_f32 v[116:117], v[116:117], v[120:121]\n"
"  v_mul_f32 v118, s61, v116\n"
"  v_exp_f32 v118, v118\n"
"  v_mul_f32 v116, v116, v117\n"
"  v_add_f32 v118, 1.0, v118\n"
"  v_rcp_f32 v118, v118\n"
"  s_nop 0\n"
"  v_mul_f32 v116, v116, v118\n"
"  s_nop 1\n"
"  v_mul_f32_dpp v120, v116, v96 row_newbcast:0 row_mask:0xf bank_mask:0xf\n"
"  v_mul_f32_dpp v121, v116, v97 row_newbcast:1 row_mask:0xf bank_mask:0xf\n"
"  v_mul_f32_dpp v122, v116, v98 row_newbcast:2 row_mask:0xf bank_mask:0xf\n"
"  v_mul_f32_dpp v123, v116, v99 row_newbcast:3 row_mask:0xf bank_mask:0xf\n"
"  v_fmac_f32_dpp v120, v116, v100 row_newbcast:4 row_mask:0xf bank_mask:0xf\n"
"  v_fmac_f32_dpp v121, v116, v101 row_newbcast:5 row_mask:0xf bank_mask:0xf\n"
"  v_fmac_f32_dpp v122, v116, v102 row_newbcast:6 row_mask:0xf bank_mask:0xf\n"
"  v_fmac_f32_dpp v123, v116, v103 row_newbcast:7 row_mask:0xf bank_mask:0xf\n"
"  v_fmac_f32_dpp v120, v116, v104 row_newbcast:8 row_mask:0xf bank_mask:0xf\n"
"  v_fmac_f32_dpp v121, v116, v105 row_newbcast:9 row_mask:0xf bank_mask:0xf\n"
"  v_fmac_f32_dpp v122, v116, v106 row_newbcast:10 row_mask:0xf bank_mask:0xf\n"
"  v_fmac_f32_dpp v123, v116, v107 row_newbcast:11 row_mask:0xf bank_mask:0xf\n"
"  v_fmac_f32_dpp v120, v116, v108 row_newbcast:12 row_mask:0xf bank_mask:0xf\n"
"  v_fmac_f32_dpp v121, v116, v109 row_newbcast:13 row_mask:0xf bank_mask:0xf\n"
"  v_fmac_f32_dpp v122, v116, v110 row_newbcast:14 row_mask:0xf bank_mask:0xf\n"
"  v_fmac_f32_dpp v123, v116, v111 row_newbcast:15 row_mask:0xf bank_mask:0xf\n"
"  v_pk_add_f32 v[120:121], v[120:121], v[122:123]\n"
"  v_add_f32 v116, v120, v121\n"
"  s_nop 1\n"
"  v_mov_b32_dpp v117, v116 quad_perm:[0,0,2,2] row_mask:0xf bank_mask:0xf\n"
"  v_mov_b32_dpp v118, v116 quad_perm:[1,1,3,3] row_mask:0xf bank_mask:0xf\n"
"  v_cvt_pk_bf16_f32 v116, v117, v118\n"
"  v_add_u32 v116, v116, v114\n"
"  buffer_store_dword v116, v113, s[20:23], s75 offen sc1\n"
"  s_branch .Lfb_L_end\n"
".Lfb_L_np:\n"
"  s_waitcnt lgkmcnt(0)\n"
"  s_barrier\n"
"  s_cmp_lg_u32 s46, 0\n"
"  s_cbranch_scc1 .Lfb_L_end\n"
"  s_lshr_b32 s60, s47, 1\n"
"  s_mul_i32 s60, s60, 7\n"
"  s_add_u32 s60, s60, s44\n"
"  s_lshl_b32 s60, s60, 5\n"
"  s_and_b32 s61, s47, 1\n"
"  s_add_u32 s60, s60, s61\n"
"  s_lshl_b32 s60, s60, 6\n"
"  s_add_u32 s75, s60, 653312\n"
"  v_lshrrev_b32 v112, 2, v255\n"
"  v_and_b32 v113, 0x30, v254\n"
"  v_lshl_or_b32 v112, v112, 7, v113\n"
"  v_bfe_u32 v114, v255, 4, 1\n"
"  v_lshrrev_b32 v113, 5, v255\n"
"  v_lshl_or_b32 v113, v114, 1, v113\n"
"  v_and_b32 v114, 3, v255\n"
"  v_lshl_or_b32 v113, v114, 2, v113\n"
"  s_lshl2_add_u32 s60, s44, 157696\n"
"  v_lshl_add_u32 v113, v113, 5, s60\n"
"  s_barrier\n"
"  s_sleep 14\n"
"  buffer_load_dwordx4 v[116:119], v112, s[20:23], s75 offen sc1\n"
".Lfb_L_acc:\n"
"  s_waitcnt vmcnt(0)\n"
"  v_sub_u32 v117, v117, v116\n"
"  v_sub_u32 v119, v119, v118\n"
"  v_lshlrev_b32 v120, 16, v116\n"
"  v_and_b32 v121, s59, v116\n"
"  v_cmp_ne_u32 vcc, s56, v117\n"
"  v_cmp_ne_u32 s[90:91], s56, v119\n"
"  v_lshlrev_b32 v122, 16, v118\n"
"  v_and_b32 v123, s59, v118\n"
"  v_permlane32_swap_b32 v120, v121\n"
"  s_or_b64 vcc, vcc, s[90:91]\n"
"  v_permlane32_swap_b32 v122, v123\n"
"  s_cbranch_vccnz .Lfb_L_acr\n"
".Lfb_L_ack:\n"
"  v_add_f32 v120, v120, v121\n"
"  v_add_f32 v122, v122, v123\n"
"  s_nop 1\n"
"  v_permlane16_swap_b32 v120, v122\n"
"  v_add_f32 v120, v120, v122\n"
"  s_nop 1\n"
"  v_add_f32_dpp v120, v120, v120 row_ror:8 row_mask:0xf bank_mask:0xf\n"
"  s_nop 1\n"
"  v_add_f32_dpp v120, v120, v120 row_ror:4 row_mask:0xf bank_mask:0xf\n"
"  ds_write_b32 v113, v120\n"
"  s_waitcnt lgkmcnt(0)\n"
"  s_barrier\n"
".Lfb_L_end:\n"
"  s_endpgm\n"
".Lfb_M:\n"
"  s_waitcnt lgkmcnt(0)\n"
"  s_load_dwordx16 s[24:39], s[0:1], 0x40\n"
"  s_load_dwordx4 s[40:43], s[0:1], 0x80\n"
"  s_load_dword s57, s[20:21], 0x0\n"
"  s_lshr_b32 s50, s47, 2\n"
"  s_and_b32 s51, s47, 3\n"
"  s_lshl3_add_u32 s52, s46, s50\n"
"  s_lshr_b32 s55, s45, 1\n"
"  s_mov_b32 s59, 0xffff0000\n"
"  s_min_u32 s3, s46, 1\n"
"  s_andn2_b32 s3, s3, s47\n"
"  v_lshlrev_b32 v253, 2, v255\n"
"  v_bfe_u32 v249, v255, 2, 2\n"
"  v_lshlrev_b32 v204, 7, v249\n"
"  v_lshrrev_b32 v249, 1, v249\n"
"  v_lshl_or_b32 v204, v249, 6, v204\n"
"  v_bfe_u32 v249, v255, 4, 1\n"
"  v_lshl_or_b32 v204, v249, 5, v204\n"
"  v_and_b32 v249, 3, v255\n"
"  v_lshl_or_b32 v204, v249, 3, v204\n"
"  s_and_b32 s54, s45, 1\n"
"  v_mov_b32 v251, v204\n"
"  v_and_b32 v0, 31, v255\n"
"  v_lshrrev_b32 v1, 5, v255\n"
"  v_bfe_u32 v2, v255, 1, 3\n"
"  v_xor_b32 v2, v2, v1\n"
"  v_lshlrev_b32 v2, 4, v2\n"
"  v_lshl_or_b32 v2, v0, 7, v2\n"
"  s_lshl_b32 s60, s54, 6\n"
"  v_xor_b32 v2, s60, v2\n"
"  s_lshl_b32 s60, s54, 12\n"
"  s_add_u32 s60, s60, 73728\n"
"  v_add_u32 v220, s60, v2\n"
"  s_lshl_b32 s60, s55, 6\n"
"  v_xor_b32 v2, s60, v204\n"
"  v_lshl_or_b32 v2, v1, 10, v2\n"
"  s_lshl_b32 s60, s54, 13\n"
"  v_or_b32 v224, s60, v2\n"
"  v_xor_b32 v221, 32, v220\n"
"  v_xor_b32 v225, 4096, v224\n"
"  v_xor_b32 v222, 64, v220\n"
"  v_xor_b32 v226, 8192, v224\n"
"  v_xor_b32 v223, 96, v220\n"
"  v_xor_b32 v227, 12288, v224\n"
"  s_lshl_b32 s60, s55, 9\n"
"  s_add_u32 s60, s60, 157696\n"
"  v_lshl_add_u32 v2, v0, 4, s60\n"
"  v_lshl_add_u32 v229, v1, 3, v2\n"
"  s_lshl_b32 s60, s54, 8\n"
"  s_add_u32 s60, s60, 156928\n"
"  v_lshl_add_u32 v230, v0, 3, s60\n"
"  s_lshl_b32 s60, s54, 7\n"
"  s_add_u32 s60, s60, 156416\n"
"  v_lshl_add_u32 v231, v0, 2, s60\n"
"  v_lshl_add_u32 v232, v1, 4, s60\n"
"  s_lshl1_add_u32 s60, s49, s55\n"
"  s_lshl_b32 s60, s60, 5\n"
"  s_add_u32 s60, s60, s48\n"
"  s_lshl1_add_u32 s60, s60, s54\n"
"  s_mul_i32 s60, s60, 4352\n"
"  s_add_u32 s60, s60, 1048576\n"
"  v_add_u32 v233, s60, v254\n"
"  s_add_u32 s60, s60, 4096\n"
"  v_add_u32 v234, s60, v253\n"
"  s_lshl1_add_u32 s60, s54, s55\n"
"  s_lshl_b32 s60, s60, 12\n"
"  s_add_u32 s60, s60, 139264\n"
"  v_add_u32 v235, s60, v254\n"
"  v_bfe_u32 v2, v255, 2, 2\n"
"  v_lshrrev_b32 v3, 4, v255\n"
"  v_cmp_eq_u32 vcc, v2, v3\n"
"  v_mov_b32 v4, 0x3f803f80\n"
"  v_and_b32 v2, 22, v255\n"
"  v_cndmask_b32 v216, 0, v4, vcc\n"
"  v_cmp_eq_u32 vcc, 0, v2\n"
"  s_lshl_b32 s60, s45, 10\n"
"  s_add_u32 s60, s60, 159744\n"
"  v_cndmask_b32 v4, 0, v4, vcc\n"
"  v_lshl_add_u32 v228, v255, 3, s60\n"
"  v_mov_b32 v5, v4\n"
"  v_xor_b32 v247, 1024, v228\n"
"  v_mov_b32 v238, 0\n"
"  v_mov_b32 v239, 0\n"
"  v_mov_b32 v217, v216\n"
"  v_mov_b32 v218, v216\n"
"  v_mov_b32 v219, v216\n"
"  v_lshlrev_b32 v2, 3, v0\n"
"  v_lshl_or_b32 v2, v1, 10, v2\n"
"  v_add_u32 v2, 155648, v2\n"
"  ds_write_b64 v2, v[4:5]\n"
"  v_and_b32 v2, 0x38, v253\n"
"  v_add_u32 v2, 155904, v2\n"
"  ds_write_b64 v2, v[238:239]\n"
"  v_mov_b32 v242, 0\n"
"  v_mov_b32 v243, 0\n"
"  v_mov_b32 v244, 0\n"
"  v_mov_b32 v245, 0\n"
"  s_waitcnt lgkmcnt(0)\n"
"  s_add_u32 s56, s57, 1\n"
"  s_mul_i32 s56, s56, 0x9E3779B1\n"
"  s_add_u32 s56, s56, 0x7F4A7C15\n"
"  s_mov_b32 s58, 0\n"
"  s_mul_i32 s89, s50, 2\n"
"  s_and_b32 s60, s45, 1\n"
"  s_lshl_b32 s88, s60, 13\n"
"  s_lshl_b32 s60, s51, 8\n"
"  s_add_u32 s88, s88, s60\n"
"  s_lshl_b32 s60, s55, 7\n"
"  s_add_u32 s88, s88, s60\n"
"  s_waitcnt lgkmcnt(0)\n"
"  s_barrier\n"
"  ds_read_b128 v[0:3], v220\n"
"  ds_read_b64_tr_b16 v[160:161], v224 offset:0\n"
"  ds_read_b64_tr_b16 v[162:163], v224 offset:512\n"
"  ds_read_b64_tr_b16 v[164:165], v224 offset:2048\n"
"  ds_read_b64_tr_b16 v[166:167], v224 offset:2560\n"
"  ds_read_b128 v[4:7], v221\n"
"  ds_read_b64_tr_b16 v[168:169], v225 offset:0\n"
"  ds_read_b64_tr_b16 v[170:171], v225 offset:512\n"
"  ds_read_b64_tr_b16 v[172:173], v225 offset:2048\n"
"  ds_read_b64_tr_b16 v[174:175], v225 offset:2560\n"
"  ds_read_b128 v[8:11], v222\n"
"  ds_read_b64_tr_b16 v[176:177], v226 offset:0\n"
"  ds_read_b64_tr_b16 v[178:179], v226 offset:512\n"
"  s_waitcnt lgkmcnt(10)\n"
"  v_mfma_f32_16x16x32_bf16 v[208:211], v[216:219], v[160:163], 0\n"
"  ds_read_b64_tr_b16 v[180:181], v226 offset:2048\n"
"  s_waitcnt lgkmcnt(9)\n"
"  v_mfma_f32_16x16x32_bf16 v[212:215], v[216:219], v[164:167], 0\n"
"  ds_read_b64_tr_b16 v[182:183], v226 offset:2560\n"
"  v_mfma_f32_32x32x16_bf16 v[128:143], v[160:163], v[0:3], 0\n"
"  ds_read_b128 v[12:15], v223\n"
"  ds_read_b64_tr_b16 v[184:185], v227 offset:0\n"
"  ds_read_b64_tr_b16 v[186:187], v227 offset:512\n"
"  v_mfma_f32_32x32x16_bf16 v[144:159], v[0:3], v[164:167], 0\n"
"  ds_read_b64_tr_b16 v[188:189], v227 offset:2048\n"
"  ds_read_b64_tr_b16 v[190:191], v227 offset:2560\n"
"  s_waitcnt lgkmcnt(12)\n"
"  v_mfma_f32_16x16x32_bf16 v[208:211], v[216:219], v[168:171], v[208:211]\n"
"  s_waitcnt lgkmcnt(10)\n"
"  v_mfma_f32_16x16x32_bf16 v[212:215], v[216:219], v[172:175], v[212:215]\n"
"  v_mfma_f32_32x32x16_bf16 v[128:143], v[168:171], v[4:7], v[128:143]\n"
"  v_mfma_f32_32x32x16_bf16 v[144:159], v[4:7], v[172:175], v[144:159]\n"
"  s_barrier\n"
"  s_waitcnt lgkmcnt(7)\n"
"  v_mfma_f32_32x32x16_bf16 v[128:143], v[176:179], v[8:11], v[128:143]\n"
"  ds_read_b128 v[16:19], v220 offset:8192\n"
"  ds_read_b64_tr_b16 v[160:161], v224 offset:16384\n"
"  ds_read_b64_tr_b16 v[162:163], v224 offset:16896\n"
"  ds_read_b64_tr_b16 v[164:165], v224 offset:18432\n"
"  s_waitcnt lgkmcnt(9)\n"
"  v_mfma_f32_32x32x16_bf16 v[144:159], v[8:11], v[180:183], v[144:159]\n"
"  ds_read_b64_tr_b16 v[166:167], v224 offset:18944\n"
"  ds_read_b128 v[20:23], v221 offset:8192\n"
"  ds_read_b64_tr_b16 v[168:169], v225 offset:16384\n"
"  s_waitcnt lgkmcnt(9)\n"
"  v_mfma_f32_32x32x16_bf16 v[128:143], v[184:187], v[12:15], v[128:143]\n"
"  ds_read_b64_tr_b16 v[170:171], v225 offset:16896\n"
"  ds_read_b64_tr_b16 v[172:173], v225 offset:18432\n"
"  ds_read_b64_tr_b16 v[174:175], v225 offset:18944\n"
"  s_waitcnt lgkmcnt(10)\n"
"  v_mfma_f32_32x32x16_bf16 v[144:159], v[12:15], v[188:191], v[144:159]\n"
"  ds_read_b128 v[24:27], v222 offset:8192\n"
"  ds_read_b64_tr_b16 v[192:193], v226 offset:16384\n"
"  ds_read_b64_tr_b16 v[194:195], v226 offset:16896\n"
"  s_waitcnt lgkmcnt(10)\n"
"  v_mfma_f32_16x16x32_bf16 v[208:211], v[216:219], v[160:163], v[208:211]\n"
"  ds_read_b64_tr_b16 v[196:197], v226 offset:18432\n"
"  s_waitcnt lgkmcnt(9)\n"
"  v_mfma_f32_16x16x32_bf16 v[212:215], v[216:219], v[164:167], v[212:215]\n"
"  ds_read_b64_tr_b16 v[198:199], v226 offset:18944\n"
"  v_mfma_f32_32x32x16_bf16 v[128:143], v[160:163], v[16:19], v[128:143]\n"
"  ds_read_b128 v[28:31], v223 offset:8192\n"
"  ds_read_b64_tr_b16 v[200:201], v227 offset:16384\n"
"  ds_read_b64_tr_b16 v[202:203], v227 offset:16896\n"
"  v_mfma_f32_32x32x16_bf16 v[144:159], v[16:19], v[164:167], v[144:159]\n"
"  ds_read_b64_tr_b16 v[204:205], v227 offset:18432\n"
"  ds_read_b64_tr_b16 v[206:207], v227 offset:18944\n"
"  s_waitcnt lgkmcnt(12)\n"
"  v_mfma_f32_16x16x32_bf16 v[208:211], v[216:219], v[168:171], v[208:211]\n"
"  s_waitcnt lgkmcnt(10)\n"
"  v_mfma_f32_16x16x32_bf16 v[212:215], v[216:219], v[172:175], v[212:215]\n"
"  v_mfma_f32_32x32x16_bf16 v[128:143], v[168:171], v[20:23], v[128:143]\n"
"  v_mfma_f32_32x32x16_bf16 v[144:159], v[20:23], v[172:175], v[144:159]\n"
"  s_barrier\n"
"  s_waitcnt lgkmcnt(7)\n"
"  v_mfma_f32_32x32x16_bf16 v[128:143], v[192:195], v[24:27], v[128:143]\n"
"  ds_read_b128 v[32:35], v220 offset:16384\n"
"  ds_read_b64_tr_b16 v[160:161], v224 offset:32768\n"
"  ds_read_b64_tr_b16 v[162:163], v224 offset:33280\n"
"  ds_read_b64_tr_b16 v[164:165], v224 offset:34816\n"
"  s_waitcnt lgkmcnt(9)\n"
"  v_mfma_f32_32x32x16_bf16 v[144:159], v[24:27], v[196:199], v[144:159]\n"
"  ds_read_b64_tr_b16 v[166:167], v224 offset:35328\n"
"  ds_read_b128 v[36:39], v221 offset:16384\n"
"  ds_read_b64_tr_b16 v[168:169], v225 offset:32768\n"
"  s_waitcnt lgkmcnt(9)\n"
"  v_mfma_f32_32x32x16_bf16 v[128:143], v[200:203], v[28:31], v[128:143]\n"
"  ds_read_b64_tr_b16 v[170:171], v225 offset:33280\n"
"  ds_read_b64_tr_b16 v[172:173], v225 offset:34816\n"
"  ds_read_b64_tr_b16 v[174:175], v225 offset:35328\n"
"  s_waitcnt lgkmcnt(10)\n"
"  v_mfma_f32_32x32x16_bf16 v[144:159], v[28:31], v[204:207], v[144:159]\n"
"  ds_read_b128 v[40:43], v222 offset:16384\n"
"  ds_read_b64_tr_b16 v[176:177], v226 offset:32768\n"
"  ds_read_b64_tr_b16 v[178:179], v226 offset:33280\n"
"  s_waitcnt lgkmcnt(10)\n"
"  v_mfma_f32_16x16x32_bf16 v[208:211], v[216:219], v[160:163], v[208:211]\n"
"  ds_read_b64_tr_b16 v[180:181], v226 offset:34816\n"
"  s_waitcnt lgkmcnt(9)\n"
"  v_mfma_f32_16x16x32_bf16 v[212:215], v[216:219], v[164:167], v[212:215]\n"
"  ds_read_b64_tr_b16 v[182:183], v226 offset:35328\n"
"  v_mfma_f32_32x32x16_bf16 v[128:143], v[160:163], v[32:35], v[128:143]\n"
"  ds_read_b128 v[44:47], v223 offset:16384\n"
"  ds_read_b64_tr_b16 v[184:185], v227 offset:32768\n"
"  ds_read_b64_tr_b16 v[186:187], v227 offset:33280\n"
"  v_mfma_f32_32x32x16_bf16 v[144:159], v[32:35], v[164:167], v[144:159]\n"
"  ds_read_b64_tr_b16 v[188:189], v227 offset:34816\n"
"  ds_read_b64_tr_b16 v[190:191], v227 offset:35328\n"
"  s_waitcnt lgkmcnt(12)\n"
"  v_mfma_f32_16x16x32_bf16 v[208:211], v[216:219], v[168:171], v[208:211]\n"
"  s_waitcnt lgkmcnt(10)\n"
"  v_mfma_f32_16x16x32_bf16 v[212:215], v[216:219], v[172:175], v[212:215]\n"
"  v_mfma_f32_32x32x16_bf16 v[128:143], v[168:171], v[36:39], v[128:143]\n"
"  v_mfma_f32_32x32x16_bf16 v[144:159], v[36:39], v[172:175], v[144:159]\n"
"  s_barrier\n"
"  s_waitcnt lgkmcnt(7)\n"
"  v_mfma_f32_32x32x16_bf16 v[128:143], v[176:179], v[40:43], v[128:143]\n"
"  ds_read_b128 v[48:51], v220 offset:24576\n"
"  ds_read_b64_tr_b16 v[160:161], v224 offset:49152\n"
"  ds_read_b64_tr_b16 v[162:163], v224 offset:49664\n"
"  ds_read_b64_tr_b16 v[164:165], v224 offset:51200\n"
"  s_waitcnt lgkmcnt(9)\n"
"  v_mfma_f32_32x32x16_bf16 v[144:159], v[40:43], v[180:183], v[144:159]\n"
"  ds_read_b64_tr_b16 v[166:167], v224 offset:51712\n"
"  ds_read_b128 v[52:55], v221 offset:24576\n"
"  ds_read_b64_tr_b16 v[168:169], v225 offset:49152\n"
"  s_waitcnt lgkmcnt(9)\n"
"  v_mfma_f32_32x32x16_bf16 v[128:143], v[184:187], v[44:47], v[128:143]\n"
"  ds_read_b64_tr_b16 v[170:171], v225 offset:49664\n"
"  ds_read_b64_tr_b16 v[172:173], v225 offset:51200\n"
"  ds_read_b64_tr_b16 v[174:175], v225 offset:51712\n"
"  s_waitcnt lgkmcnt(10)\n"
"  v_mfma_f32_32x32x16_bf16 v[144:159], v[44:47], v[188:191], v[144:159]\n"
"  ds_read_b128 v[56:59], v222 offset:24576\n"
"  ds_read_b64_tr_b16 v[192:193], v226 offset:49152\n"
"  ds_read_b64_tr_b16 v[194:195], v226 offset:49664\n"
"  s_waitcnt lgkmcnt(10)\n"
"  v_mfma_f32_16x16x32_bf16 v[208:211], v[216:219], v[160:163], v[208:211]\n"
"  ds_read_b64_tr_b16 v[196:197], v226 offset:51200\n"
"  s_waitcnt lgkmcnt(9)\n"
"  v_mfma_f32_16x16x32_bf16 v[212:215], v[216:219], v[164:167], v[212:215]\n"
"  ds_read_b64_tr_b16 v[198:199], v226 offset:51712\n"
"  v_mfma_f32_32x32x16_bf16 v[128:143], v[160:163], v[48:51], v[128:143]\n"
"  ds_read_b128 v[60:63], v223 offset:24576\n"
"  ds_read_b64_tr_b16 v[200:201], v227 offset:49152\n"
"  ds_read_b64_tr_b16 v[202:203], v227 offset:49664\n"
"  v_mfma_f32_32x32x16_bf16 v[144:159], v[48:51], v[164:167], v[144:159]\n"
"  ds_read_b64_tr_b16 v[204:205], v227 offset:51200\n"
"  ds_read_b64_tr_b16 v[206:207], v227 offset:51712\n"
"  s_waitcnt lgkmcnt(12)\n"
"  v_mfma_f32_16x16x32_bf16 v[208:211], v[216:219], v[168:171], v[208:211]\n"
"  s_waitcnt lgkmcnt(10)\n"
"  v_mfma_f32_16x16x32_bf16 v[212:215], v[216:219], v[172:175], v[212:215]\n"
"  v_mfma_f32_32x32x16_bf16 v[128:143], v[168:171], v[52:55], v[128:143]\n"
"  v_mfma_f32_32x32x16_bf16 v[144:159], v[52:55], v[172:175], v[144:159]\n"
"  s_barrier\n"
"  s_waitcnt lgkmcnt(7)\n"
"  v_mfma_f32_32x32x16_bf16 v[128:143], v[192:195], v[56:59], v[128:143]\n"
"  ds_read_b128 v[64:67], v220 offset:32768\n"
"  ds_read_b64_tr_b16 v[160:161], v224 offset:0\n"
"  ds_read_b64_tr_b16 v[162:163], v224 offset:512\n"
"  ds_read_b64_tr_b16 v[164:165], v224 offset:2048\n"
"  s_waitcnt lgkmcnt(9)\n"
"  v_mfma_f32_32x32x16_bf16 v[144:159], v[56:59], v[196:199], v[144:159]\n"
"  ds_read_b64_tr_b16 v[166:167], v224 offset:2560\n"
"  ds_read_b128 v[68:71], v221 offset:32768\n"
"  ds_read_b64_tr_b16 v[168:169], v225 offset:0\n"
"  s_waitcnt lgkmcnt(9)\n"
"  v_mfma_f32_32x32x16_bf16 v[128:143], v[200:203], v[60:63], v[128:143]\n"
"  ds_read_b64_tr_b16 v[170:171], v225 offset:512\n"
"  ds_read_b64_tr_b16 v[172:173], v225 offset:2048\n"
"  ds_read_b64_tr_b16 v[174:175], v225 offset:2560\n"
"  s_waitcnt lgkmcnt(10)\n"
"  v_mfma_f32_32x32x16_bf16 v[144:159], v[60:63], v[204:207], v[144:159]\n"
"  ds_read_b128 v[72:75], v222 offset:32768\n"
"  ds_read_b64_tr_b16 v[176:177], v226 offset:0\n"
"  ds_read_b64_tr_b16 v[178:179], v226 offset:512\n"
"  s_waitcnt lgkmcnt(10)\n"
"  v_mfma_f32_16x16x32_bf16 v[208:211], v[216:219], v[160:163], v[208:211]\n"
"  ds_read_b64_tr_b16 v[180:181], v226 offset:2048\n"
"  s_waitcnt lgkmcnt(9)\n"
"  v_mfma_f32_16x16x32_bf16 v[212:215], v[216:219], v[164:167], v[212:215]\n"
"  ds_read_b64_tr_b16 v[182:183], v226 offset:2560\n"
"  v_mfma_f32_32x32x16_bf16 v[128:143], v[160:163], v[64:67], v[128:143]\n"
"  ds_read_b128 v[76:79], v223 offset:32768\n"
"  ds_read_b64_tr_b16 v[184:185], v227 offset:0\n"
"  ds_read_b64_tr_b16 v[186:187], v227 offset:512\n"
"  v_mfma_f32_32x32x16_bf16 v[144:159], v[64:67], v[164:167], v[144:159]\n"
"  ds_read_b64_tr_b16 v[188:189], v227 offset:2048\n"
"  ds_read_b64_tr_b16 v[190:191], v227 offset:2560\n"
"  s_waitcnt lgkmcnt(12)\n"
"  v_mfma_f32_16x16x32_bf16 v[208:211], v[216:219], v[168:171], v[208:211]\n"
"  s_waitcnt lgkmcnt(10)\n"
"  v_mfma_f32_16x16x32_bf16 v[212:215], v[216:219], v[172:175], v[212:215]\n"
"  v_mfma_f32_32x32x16_bf16 v[128:143], v[168:171], v[68:71], v[128:143]\n"
"  v_mfma_f32_32x32x16_bf16 v[144:159], v[68:71], v[172:175], v[144:159]\n"
"  s_barrier\n"
"  s_waitcnt lgkmcnt(7)\n"
"  v_mfma_f32_32x32x16_bf16 v[128:143], v[176:179], v[72:75], v[128:143]\n"
"  ds_read_b128 v[80:83], v220 offset:40960\n"
"  ds_read_b64_tr_b16 v[160:161], v224 offset:16384\n"
"  ds_read_b64_tr_b16 v[162:163], v224 offset:16896\n"
"  ds_read_b64_tr_b16 v[164:165], v224 offset:18432\n"
"  s_waitcnt lgkmcnt(9)\n"
"  v_mfma_f32_32x32x16_bf16 v[144:159], v[72:75], v[180:183], v[144:159]\n"
"  ds_read_b64_tr_b16 v[166:167], v224 offset:18944\n"
"  ds_read_b128 v[84:87], v221 offset:40960\n"
"  ds_read_b64_tr_b16 v[168:169], v225 offset:16384\n"
"  s_waitcnt lgkmcnt(9)\n"
"  v_mfma_f32_32x32x16_bf16 v[128:143], v[184:187], v[76:79], v[128:143]\n"
"  ds_read_b64_tr_b16 v[170:171], v225 offset:16896\n"
"  ds_read_b64_tr_b16 v[172:173], v225 offset:18432\n"
"  ds_read_b64_tr_b16 v[174:175], v225 offset:18944\n"
"  s_waitcnt lgkmcnt(10)\n"
"  v_mfma_f32_32x32x16_bf16 v[144:159], v[76:79], v[188:191], v[144:159]\n"
"  ds_read_b128 v[88:91], v222 offset:40960\n"
"  ds_read_b64_tr_b16 v[192:193], v226 offset:16384\n"
"  ds_read_b64_tr_b16 v[194:195], v226 offset:16896\n"
"  s_waitcnt lgkmcnt(10)\n"
"  v_mfma_f32_16x16x32_bf16 v[208:211], v[216:219], v[160:163], v[208:211]\n"
"  ds_read_b64_tr_b16 v[196:197], v226 offset:18432\n"
"  s_waitcnt lgkmcnt(9)\n"
"  v_mfma_f32_16x16x32_bf16 v[212:215], v[216:219], v[164:167], v[212:215]\n"
"  ds_read_b64_tr_b16 v[198:199], v226 offset:18944\n"
"  v_mfma_f32_32x32x16_bf16 v[128:143], v[160:163], v[80:83], v[128:143]\n"
"  ds_read_b128 v[92:95], v223 offset:40960\n"
"  ds_read_b64_tr_b16 v[200:201], v227 offset:16384\n"
"  ds_read_b64_tr_b16 v[202:203], v227 offset:16896\n"
"  v_mfma_f32_32x32x16_bf16 v[144:159], v[80:83], v[164:167], v[144:159]\n"
"  ds_read_b64_tr_b16 v[204:205], v227 offset:18432\n"
"  ds_read_b64_tr_b16 v[206:207], v227 offset:18944\n"
"  s_waitcnt lgkmcnt(12)\n"
"  v_mfma_f32_16x16x32_bf16 v[208:211], v[216:219], v[168:171], v[208:211]\n"
"  s_waitcnt lgkmcnt(10)\n"
"  v_mfma_f32_16x16x32_bf16 v[212:215], v[216:219], v[172:175], v[212:215]\n"
"  v_mfma_f32_32x32x16_bf16 v[128:143], v[168:171], v[84:87], v[128:143]\n"
"  v_mfma_f32_32x32x16_bf16 v[144:159], v[84:87], v[172:175], v[144:159]\n"
"  s_barrier\n"
"  s_waitcnt lgkmcnt(7)\n"
"  v_mfma_f32_32x32x16_bf16 v[128:143], v[192:195], v[88:91], v[128:143]\n"
"  ds_read_b128 v[96:99], v220 offset:49152\n"
"  ds_read_b64_tr_b16 v[160:161], v224 offset:32768\n"
"  ds_read_b64_tr_b16 v[162:163], v224 offset:33280\n"
"  ds_read_b64_tr_b16 v[164:165], v224 offset:34816\n"
"  s_waitcnt lgkmcnt(9)\n"
"  v_mfma_f32_32x32x16_bf16 v[144:159], v[88:91], v[196:199], v[144:159]\n"
"  ds_read_b64_tr_b16 v[166:167], v224 offset:35328\n"
"  ds_read_b128 v[100:103], v221 offset:49152\n"
"  ds_read_b64_tr_b16 v[168:169], v225 offset:32768\n"
"  ds_read_b64_tr_b16 v[170:171], v225 offset:33280\n"
"  s_waitcnt lgkmcnt(10)\n"
"  v_mfma_f32_32x32x16_bf16 v[128:143], v[200:203], v[92:95], v[128:143]\n"
"  ds_read_b64_tr_b16 v[172:173], v225 offset:34816\n"
"  ds_read_b64_tr_b16 v[174:175], v225 offset:35328\n"
"  ds_read_b128 v[104:107], v222 offset:49152\n"
"  ds_read_b64_tr_b16 v[176:177], v226 offset:32768\n"
"  s_waitcnt lgkmcnt(12)\n"
"  v_mfma_f32_32x32x16_bf16 v[144:159], v[92:95], v[204:207], v[144:159]\n"
"  ds_read_b64_tr_b16 v[178:179], v226 offset:33280\n"
"  ds_read_b64_tr_b16 v[180:181], v226 offset:34816\n"
"  ds_read_b64_tr_b16 v[182:183], v226 offset:35328\n"
"  ds_read_b128 v[108:111], v223 offset:49152\n"
"  s_waitcnt lgkmcnt(13)\n"
"  v_mfma_f32_16x16x32_bf16 v[208:211], v[216:219], v[160:163], v[208:211]\n"
"  ds_read_b64_tr_b16 v[184:185], v227 offset:32768\n"
"  s_waitcnt lgkmcnt(12)\n"
"  v_mfma_f32_16x16x32_bf16 v[212:215], v[216:219], v[164:167], v[212:215]\n"
"  ds_read_b64_tr_b16 v[186:187], v227 offset:33280\n"
"  s_waitcnt lgkmcnt(10)\n"
"  v_mfma_f32_16x16x32_bf16 v[208:211], v[216:219], v[168:171], v[208:211]\n"
"  ds_read_b64_tr_b16 v[188:189], v227 offset:34816\n"
"  s_waitcnt lgkmcnt(9)\n"
"  v_mfma_f32_16x16x32_bf16 v[212:215], v[216:219], v[172:175], v[212:215]\n"
"  ds_read_b64_tr_b16 v[190:191], v227 offset:35328\n"
"  v_mfma_f32_32x32x16_bf16 v[128:143], v[160:163], v[96:99], v[128:143]\n"
"  v_mfma_f32_32x32x16_bf16 v[144:159], v[96:99], v[164:167], v[144:159]\n"
"  s_nop 4\n"
"  ds_write2_b32 v228, v208, v212 offset0:0 offset1:1\n"
"  v_mfma_f32_32x32x16_bf16 v[128:143], v[168:171], v[100:103], v[128:143]\n"
"  v_mfma_f32_32x32x16_bf16 v[144:159], v[100:103], v[172:175], v[144:159]\n"
"  s_waitcnt lgkmcnt(0)\n"
"  s_barrier\n"
"  v_mfma_f32_32x32x16_bf16 v[128:143], v[176:179], v[104:107], v[128:143]\n"
"  ds_read_b128 v[112:115], v220 offset:57344\n"
"  ds_read_b64_tr_b16 v[160:161], v224 offset:49152\n"
"  ds_read_b64_tr_b16 v[162:163], v224 offset:49664\n"
"  ds_read_b64_tr_b16 v[164:165], v224 offset:51200\n"
"  ds_read_b64_tr_b16 v[166:167], v224 offset:51712\n"
"  v_mfma_f32_32x32x16_bf16 v[144:159], v[104:107], v[180:183], v[144:159]\n"
"  ds_read_b128 v[116:119], v221 offset:57344\n"
"  ds_read_b64_tr_b16 v[168:169], v225 offset:49152\n"
"  ds_read_b64_tr_b16 v[170:171], v225 offset:49664\n"
"  ds_read_b64_tr_b16 v[172:173], v225 offset:51200\n"
"  ds_read_b64_tr_b16 v[174:175], v225 offset:51712\n"
"  v_mfma_f32_32x32x16_bf16 v[128:143], v[184:187], v[108:111], v[128:143]\n"
"  ds_read_b128 v[120:123], v222 offset:57344\n"
"  ds_read_b64_tr_b16 v[192:193], v226 offset:49152\n"
"  ds_read_b64_tr_b16 v[194:195], v226 offset:49664\n"
"  ds_read_b64_tr_b16 v[196:197], v226 offset:51200\n"
"  ds_read_b64_tr_b16 v[198:199], v226 offset:51712\n"
"  v_mfma_f32_32x32x16_bf16 v[144:159], v[108:111], v[188:191], v[144:159]\n"
"  ds_read_b128 v[124:127], v223 offset:57344\n"
"  ds_read_b64_tr_b16 v[200:201], v227 offset:49152\n"
"  ds_read_b64_tr_b16 v[202:203], v227 offset:49664\n"
"  ds_read_b64_tr_b16 v[204:205], v227 offset:51200\n"
"  ds_read_b64_tr_b16 v[206:207], v227 offset:51712\n"
"  ds_read_b64 v[244:245], v247\n"
"  s_waitcnt lgkmcnt(15)\n"
"  v_mfma_f32_16x16x32_bf16 v[208:211], v[216:219], v[160:163], v[208:211]\n"
"  s_waitcnt lgkmcnt(13)\n"
"  v_mfma_f32_16x16x32_bf16 v[208:211], v[216:219], v[168:171], v[208:211]\n"
"  s_waitcnt lgkmcnt(8)\n"
"  v_mfma_f32_16x16x32_bf16 v[208:211], v[216:219], v[192:195], v[208:211]\n"
"  s_waitcnt lgkmcnt(3)\n"
"  v_mfma_f32_16x16x32_bf16 v[208:211], v[216:219], v[200:203], v[208:211]\n"
"  s_waitcnt lgkmcnt(0)\n"
"  s_barrier\n"
"  ds_read_b64 v[236:237], v230\n"
"  ds_read_b32 v246, v231\n"
"  v_mfma_f32_32x32x16_bf16 v[128:143], v[160:163], v[112:115], v[128:143]\n"
"  ds_read_b128 v[176:179], v232\n"
"  ds_read_b128 v[180:183], v232 offset:32\n"
"  ds_read_b128 v[184:187], v232 offset:64\n"
"  ds_read_b128 v[188:191], v232 offset:96\n"
"  v_mfma_f32_32x32x16_bf16 v[128:143], v[168:171], v[116:119], v[128:143]\n"
"  v_add_f32 v208, v208, v244\n"
"  v_mfma_f32_32x32x16_bf16 v[128:143], v[192:195], v[120:123], v[128:143]\n"
"  v_cvt_pk_bf16_f32 v240, -v208, -v208\n"
"  v_lshlrev_b32 v249, 16, v240\n"
"  v_sub_f32 v249, -v208, v249\n"
"  v_cvt_pk_bf16_f32 v241, v249, v249\n"
"  v_mfma_f32_32x32x16_bf16 v[128:143], v[200:203], v[124:127], v[128:143]\n"
"  s_waitcnt lgkmcnt(5)\n"
"  v_mfma_f32_32x32x16_bf16 v[128:143], v[240:243], v[236:239], v[128:143]\n"
"  v_mfma_f32_16x16x32_bf16 v[212:215], v[216:219], v[164:167], v[212:215]\n"
"  v_mfma_f32_16x16x32_bf16 v[212:215], v[216:219], v[172:175], v[212:215]\n"
"  v_mfma_f32_16x16x32_bf16 v[212:215], v[216:219], v[196:199], v[212:215]\n"
"  v_mfma_f32_16x16x32_bf16 v[212:215], v[216:219], v[204:207], v[212:215]\n"
"  v_mfma_f32_32x32x16_bf16 v[144:159], v[112:115], v[164:167], v[144:159]\n"
"  v_mfma_f32_32x32x16_bf16 v[144:159], v[116:119], v[172:175], v[144:159]\n"
"  s_waitcnt lgkmcnt(4)\n"
"  s_nop 4\n"
"  v_mul_f32 v128, v246, v128\n"
"  v_mul_f32 v129, v246, v129\n"
"  v_cvt_pk_bf16_f32 v160, v128, v129\n"
"  v_mul_f32 v130, v246, v130\n"
"  v_mul_f32 v131, v246, v131\n"
"  v_cvt_pk_bf16_f32 v161, v130, v131\n"
"  v_mfma_f32_32x32x16_bf16 v[144:159], v[120:123], v[196:199], v[144:159]\n"
"  v_add_f32 v212, v212, v245\n"
"  v_mov_b64 v[244:245], 0\n"
"  v_cvt_pk_bf16_f32 v242, -v212, -v212\n"
"  v_lshlrev_b32 v249, 16, v242\n"
"  v_sub_f32 v249, -v212, v249\n"
"  v_cvt_pk_bf16_f32 v243, v249, v249\n"
"  v_mfma_f32_32x32x16_bf16 v[144:159], v[124:127], v[204:207], v[144:159]\n"
"  v_mul_f32 v132, v246, v132\n"
"  v_mul_f32 v133, v246, v133\n"
"  v_cvt_pk_bf16_f32 v162, v132, v133\n"
"  v_mul_f32 v134, v246, v134\n"
"  v_mul_f32 v135, v246, v135\n"
"  v_cvt_pk_bf16_f32 v163, v134, v135\n"
"  v_mfma_f32_32x32x16_bf16 v[144:159], v[236:239], v[242:245], v[144:159]\n"
"  s_barrier\n"
"  buffer_store_dwordx4 v[160:163], v233, s[20:23], 0 offen sc1\n"
"  v_mul_f32 v136, v246, v136\n"
"  v_mul_f32 v137, v246, v137\n"
"  v_cvt_pk_bf16_f32 v164, v136, v137\n"
"  v_mul_f32 v138, v246, v138\n"
"  v_mul_f32 v139, v246, v139\n"
"  v_cvt_pk_bf16_f32 v165, v138, v139\n"
"  v_mul_f32 v140, v246, v140\n"
"  v_mul_f32 v141, v246, v141\n"
"  v_cvt_pk_bf16_f32 v166, v140, v141\n"
"  v_mul_f32 v142, v246, v142\n"
"  v_mul_f32 v143, v246, v143\n"
"  v_cvt_pk_bf16_f32 v167, v142, v143\n"
"  buffer_store_dwordx4 v[164:167], v233, s[20:23], 0 offen offset:1024 sc1\n"
"  s_waitcnt lgkmcnt(3)\n"
"  v_pk_mul_f32 v[144:145], v[144:145], v[176:177]\n"
"  v_add3_u32 v248, v160, v161, s56\n"
"  v_cvt_pk_bf16_f32 v168, v144, v145\n"
"  v_pk_mul_f32 v[146:147], v[146:147], v[178:179]\n"
"  v_add3_u32 v248, v162, v163, v248\n"
"  v_cvt_pk_bf16_f32 v169, v146, v147\n"
"  s_waitcnt lgkmcnt(2)\n"
"  v_pk_mul_f32 v[148:149], v[148:149], v[180:181]\n"
"  v_add3_u32 v248, v164, v165, v248\n"
"  v_cvt_pk_bf16_f32 v170, v148, v149\n"
"  v_pk_mul_f32 v[150:151], v[150:151], v[182:183]\n"
"  v_add3_u32 v248, v166, v167, v248\n"
"  v_cvt_pk_bf16_f32 v171, v150, v151\n"
"  buffer_store_dwordx4 v[168:171], v233, s[20:23], 0 offen offset:2048 sc1\n"
"  s_waitcnt lgkmcnt(1)\n"
"  v_pk_mul_f32 v[152:153], v[152:153], v[184:185]\n"
"  v_cvt_pk_bf16_f32 v172, v152, v153\n"
"  v_pk_mul_f32 v[154:155], v[154:155], v[186:187]\n"
"  v_cvt_pk_bf16_f32 v173, v154, v155\n"
"  s_waitcnt lgkmcnt(0)\n"
"  v_pk_mul_f32 v[156:157], v[156:157], v[188:189]\n"
"  v_add3_u32 v248, v168, v169, v248\n"
"  v_cvt_pk_bf16_f32 v174, v156, v157\n"
"  v_pk_mul_f32 v[158:159], v[158:159], v[190:191]\n"
"  v_add3_u32 v248, v170, v171, v248\n"
"  v_cvt_pk_bf16_f32 v175, v158, v159\n"
"  v_add3_u32 v248, v172, v173, v248\n"
"  buffer_store_dwordx4 v[172:175], v233, s[20:23], 0 offen offset:3072 sc1\n"
"  v_add3_u32 v248, v174, v175, v248\n"
"  buffer_store_dword v248, v234, s[20:23], 0 offen sc1\n"
"  v_add_u32 v220, 73728, v224\n"
"  v_add_u32 v221, 73728, v225\n"
"  v_add_u32 v222, 73728, v226\n"
"  v_add_u32 v223, 73728, v227\n"
"  ds_write_b128 v235, v[160:163]\n"
"  ds_write_b128 v235, v[164:167] offset:1024\n"
"  ds_write_b128 v235, v[168:171] offset:2048\n"
"  ds_write_b128 v235, v[172:175] offset:3072\n"
"  s_barrier\n"
"  ds_read_b64_tr_b16 v[144:145], v220\n"
"  ds_read_b64_tr_b16 v[146:147], v220 offset:512\n"
"  ds_read_b64_tr_b16 v[148:149], v221\n"
"  ds_read_b64_tr_b16 v[150:151], v221 offset:512\n"
"  ds_read_b64_tr_b16 v[152:153], v222\n"
"  ds_read_b64_tr_b16 v[154:155], v222 offset:512\n"
"  ds_read_b64_tr_b16 v[156:157], v223\n"
"  ds_read_b64_tr_b16 v[158:159], v223 offset:512\n"
"  ds_read_b64_tr_b16 v[176:177], v220 offset:2048\n"
"  ds_read_b64_tr_b16 v[178:179], v220 offset:2560\n"
"  s_waitcnt lgkmcnt(8)\n"
"  v_mfma_f32_32x32x16_bf16 v[128:143], v[144:147], v[0:3], 0\n"
"  v_mov_b32 v204, v251\n"
"  s_lshl_b32 s60, s55, 12\n"
"  ds_read_b64_tr_b16 v[180:181], v221 offset:2048\n"
"  ds_read_b64_tr_b16 v[182:183], v221 offset:2560\n"
"  s_waitcnt lgkmcnt(8)\n"
"  v_mfma_f32_32x32x16_bf16 v[128:143], v[148:151], v[4:7], v[128:143]\n"
"  s_add_u32 s60, s60, 73728\n"
"  v_add_u32 v200, s60, v254\n"
"  ds_read_b64_tr_b16 v[184:185], v222 offset:2048\n"
"  ds_read_b64_tr_b16 v[186:187], v222 offset:2560\n"
"  s_waitcnt lgkmcnt(8)\n"
"  v_mfma_f32_32x32x16_bf16 v[128:143], v[152:155], v[8:11], v[128:143]\n"
"  v_and_b32 v249, 0x2f0, v254\n"
"  s_add_u32 s60, s60, 2048\n"
"  ds_read_b64_tr_b16 v[188:189], v223 offset:2048\n"
"  ds_read_b64_tr_b16 v[190:191], v223 offset:2560\n"
"  s_waitcnt lgkmcnt(8)\n"
"  v_mfma_f32_32x32x16_bf16 v[128:143], v[156:159], v[12:15], v[128:143]\n"
"  v_add_u32 v249, s60, v249\n"
"  v_and_b32 v250, 0xf0, v254\n"
"  ds_read_b64_tr_b16 v[144:145], v220 offset:16384\n"
"  ds_read_b64_tr_b16 v[146:147], v220 offset:16896\n"
"  s_waitcnt lgkmcnt(8)\n"
"  v_mfma_f32_32x32x16_bf16 v[128:143], v[176:179], v[16:19], v[128:143]\n"
"  v_add_u32 v250, 155648, v250\n"
"  v_and_b32 v202, 16, v255\n"
"  ds_read_b64_tr_b16 v[148:149], v221 offset:16384\n"
"  ds_read_b64_tr_b16 v[150:151], v221 offset:16896\n"
"  s_waitcnt lgkmcnt(8)\n"
"  v_mfma_f32_32x32x16_bf16 v[128:143], v[180:183], v[20:23], v[128:143]\n"
"  v_cmp_eq_u32 vcc, 0, v202\n"
"  v_xor_b32 v202, 16, v202\n"
"  ds_read_b64_tr_b16 v[152:153], v222 offset:16384\n"
"  ds_read_b64_tr_b16 v[154:155], v222 offset:16896\n"
"  s_waitcnt lgkmcnt(8)\n"
"  v_mfma_f32_32x32x16_bf16 v[128:143], v[184:187], v[24:27], v[128:143]\n"
"  v_lshlrev_b32 v202, 9, v202\n"
"  v_cndmask_b32 v203, v250, v249, vcc\n"
"  ds_read_b64_tr_b16 v[156:157], v223 offset:16384\n"
"  ds_read_b64_tr_b16 v[158:159], v223 offset:16896\n"
"  s_waitcnt lgkmcnt(8)\n"
"  v_mfma_f32_32x32x16_bf16 v[128:143], v[188:191], v[28:31], v[128:143]\n"
"  v_lshrrev_b32 v249, 5, v255\n"
"  v_and_b32 v205, 31, v255\n"
"  ds_read_b64_tr_b16 v[176:177], v220 offset:18432\n"
"  ds_read_b64_tr_b16 v[178:179], v220 offset:18944\n"
"  s_waitcnt lgkmcnt(8)\n"
"  v_mfma_f32_32x32x16_bf16 v[128:143], v[144:147], v[32:35], v[128:143]\n"
"  v_lshlrev_b32 v249, 2, v249\n"
"  v_sub_u32 v205, v205, v249\n"
"  ds_read_b64_tr_b16 v[180:181], v221 offset:18432\n"
"  ds_read_b64_tr_b16 v[182:183], v221 offset:18944\n"
"  s_waitcnt lgkmcnt(8)\n"
"  v_mfma_f32_32x32x16_bf16 v[128:143], v[148:151], v[36:39], v[128:143]\n"
"  s_add_u32 s60, s54, 8\n"
"  s_lshl_b32 s61, s60, 13\n"
"  ds_read_b64_tr_b16 v[184:185], v222 offset:18432\n"
"  ds_read_b64_tr_b16 v[186:187], v222 offset:18944\n"
"  s_waitcnt lgkmcnt(8)\n"
"  v_mfma_f32_32x32x16_bf16 v[128:143], v[152:155], v[40:43], v[128:143]\n"
"  v_add_u32 v234, s61, v200\n"
"  v_mad_u32_u24 v201, v202, s60, v203\n"
"  ds_read_b64_tr_b16 v[188:189], v223 offset:18432\n"
"  ds_read_b64_tr_b16 v[190:191], v223 offset:18944\n"
"  s_waitcnt lgkmcnt(8)\n"
"  v_mfma_f32_32x32x16_bf16 v[128:143], v[156:159], v[44:47], v[128:143]\n"
"  v_add_u32 v233, 0x10000, v200\n"
"  s_lshl_b32 s60, s54, 6\n"
"  ds_read_b64_tr_b16 v[144:145], v224\n"
"  ds_read_b64_tr_b16 v[146:147], v224 offset:512\n"
"  s_waitcnt lgkmcnt(8)\n"
"  v_mfma_f32_32x32x16_bf16 v[128:143], v[176:179], v[48:51], v[128:143]\n"
"  v_xor_b32 v206, s60, v204\n"
"  v_lshrrev_b32 v249, 5, v255\n"
"  ds_read_b64_tr_b16 v[148:149], v225\n"
"  ds_read_b64_tr_b16 v[150:151], v225 offset:512\n"
"  s_waitcnt lgkmcnt(8)\n"
"  v_mfma_f32_32x32x16_bf16 v[128:143], v[180:183], v[52:55], v[128:143]\n"
"  v_lshl_or_b32 v206, v249, 9, v206\n"
"  v_lshl_or_b32 v206, s55, 11, v206\n"
"  ds_read_b64_tr_b16 v[152:153], v226\n"
"  ds_read_b64_tr_b16 v[154:155], v226 offset:512\n"
"  s_waitcnt lgkmcnt(8)\n"
"  v_mfma_f32_32x32x16_bf16 v[128:143], v[184:187], v[56:59], v[128:143]\n"
"  s_lshl_b32 s60, s45, 10\n"
"  s_add_u32 s60, s60, 159744\n"
"  ds_read_b64_tr_b16 v[156:157], v227\n"
"  ds_read_b64_tr_b16 v[158:159], v227 offset:512\n"
"  s_waitcnt lgkmcnt(8)\n"
"  v_mfma_f32_32x32x16_bf16 v[128:143], v[188:191], v[60:63], v[128:143]\n"
"  v_add_u32 v228, s60, v254\n"
"  v_and_b32 v249, 16, v255\n"
"  s_barrier\n"
"  ds_read_b64 v[208:209], v229\n"
"  ds_read_b64_tr_b16 v[176:177], v224 offset:2048\n"
"  ds_read_b64_tr_b16 v[178:179], v224 offset:2560\n"
"  s_waitcnt lgkmcnt(9)\n"
"  v_mfma_f32_32x32x16_bf16 v[128:143], v[144:147], v[64:67], v[128:143]\n"
"  v_lshlrev_b32 v249, 2, v249\n"
"  v_and_b32 v207, 0x70, v254\n"
"  ds_read_b64_tr_b16 v[180:181], v225 offset:2048\n"
"  ds_read_b64_tr_b16 v[182:183], v225 offset:2560\n"
"  s_waitcnt lgkmcnt(9)\n"
"  v_mfma_f32_32x32x16_bf16 v[128:143], v[148:151], v[68:71], v[128:143]\n"
"  v_xor_b32 v207, v207, v249\n"
"  v_lshrrev_b32 v249, 3, v255\n"
"  ds_read_b64_tr_b16 v[184:185], v226 offset:2048\n"
"  ds_read_b64_tr_b16 v[186:187], v226 offset:2560\n"
"  s_waitcnt lgkmcnt(9)\n"
"  v_mfma_f32_32x32x16_bf16 v[128:143], v[152:155], v[72:75], v[128:143]\n"
"  v_lshl_or_b32 v207, v249, 10, v207\n"
"  v_mul_f32 v246, 0x3eb8aa3b, v246\n"
"  ds_read_b64_tr_b16 v[188:189], v227 offset:2048\n"
"  ds_read_b64_tr_b16 v[190:191], v227 offset:2560\n"
"  s_waitcnt lgkmcnt(9)\n"
"  v_mfma_f32_32x32x16_bf16 v[128:143], v[156:159], v[76:79], v[128:143]\n"
"  v_add_u32 v247, 65536, v254\n"
"  v_mov_b64 v[64:65], 0\n"
"  ds_read_b64_tr_b16 v[144:145], v224 offset:16384\n"
"  ds_read_b64_tr_b16 v[146:147], v224 offset:16896\n"
"  s_waitcnt lgkmcnt(8)\n"
"  v_mfma_f32_32x32x16_bf16 v[128:143], v[176:179], v[80:83], v[128:143]\n"
"  v_mov_b64 v[66:67], 0\n"
"  v_mov_b64 v[68:69], 0\n"
"  ds_read_b64_tr_b16 v[148:149], v225 offset:16384\n"
"  ds_read_b64_tr_b16 v[150:151], v225 offset:16896\n"
"  s_waitcnt lgkmcnt(8)\n"
"  v_mfma_f32_32x32x16_bf16 v[128:143], v[180:183], v[84:87], v[128:143]\n"
"  v_mov_b64 v[70:71], 0\n"
"  v_mov_b64 v[72:73], 0\n"
"  ds_read_b64_tr_b16 v[152:153], v226 offset:16384\n"
"  ds_read_b64_tr_b16 v[154:155], v226 offset:16896\n"
"  s_waitcnt lgkmcnt(8)\n"
"  v_mfma_f32_32x32x16_bf16 v[128:143], v[184:187], v[88:91], v[128:143]\n"
"  v_mov_b64 v[74:75], 0\n"
"  v_mov_b64 v[76:77], 0\n"
"  ds_read_b64_tr_b16 v[156:157], v227 offset:16384\n"
"  ds_read_b64_tr_b16 v[158:159], v227 offset:16896\n"
"  s_waitcnt lgkmcnt(8)\n"
"  v_mfma_f32_32x32x16_bf16 v[128:143], v[188:191], v[92:95], v[128:143]\n"
"  v_mov_b64 v[78:79], 0\n"
"  ds_read_b64_tr_b16 v[176:177], v224 offset:18432\n"
"  ds_read_b64_tr_b16 v[178:179], v224 offset:18944\n"
"  s_waitcnt lgkmcnt(8)\n"
"  v_mfma_f32_32x32x16_bf16 v[128:143], v[144:147], v[96:99], v[128:143]\n"
"  ds_read_b64_tr_b16 v[180:181], v225 offset:18432\n"
"  ds_read_b64_tr_b16 v[182:183], v225 offset:18944\n"
"  v_add_f32 v208, v208, v209\n"
"  v_cvt_pk_bf16_f32 v240, -v208, -v208\n"
"  v_lshlrev_b32 v210, 16, v240\n"
"  v_sub_f32 v210, -v208, v210\n"
"  v_cvt_pk_bf16_f32 v241, v210, v210\n"
"  s_waitcnt lgkmcnt(8)\n"
"  v_mfma_f32_32x32x16_bf16 v[128:143], v[148:151], v[100:103], v[128:143]\n"
"  ds_read_b64_tr_b16 v[184:185], v226 offset:18432\n"
"  ds_read_b64_tr_b16 v[186:187], v226 offset:18944\n"
"  s_waitcnt lgkmcnt(8)\n"
"  v_mfma_f32_32x32x16_bf16 v[128:143], v[152:155], v[104:107], v[128:143]\n"
"  ds_read_b64_tr_b16 v[188:189], v227 offset:18432\n"
"  ds_read_b64_tr_b16 v[190:191], v227 offset:18944\n"
"  v_mfma_f32_32x32x16_bf16 v[128:143], v[240:243], v[236:239], v[128:143]\n"
"  s_waitcnt lgkmcnt(8)\n"
"  v_mfma_f32_32x32x16_bf16 v[128:143], v[156:159], v[108:111], v[128:143]\n"
"  ds_read_b128 v[16:19], v247\n"
"  s_waitcnt lgkmcnt(7)\n"
"  v_mfma_f32_32x32x16_bf16 v[128:143], v[176:179], v[112:115], v[128:143]\n"
"  ds_read_b128 v[20:23], v247 offset:1024\n"
"  s_waitcnt lgkmcnt(6)\n"
"  v_mfma_f32_32x32x16_bf16 v[128:143], v[180:183], v[116:119], v[128:143]\n"
"  ds_read_b128 v[24:27], v247 offset:2048\n"
"  s_waitcnt lgkmcnt(5)\n"
"  v_mfma_f32_32x32x16_bf16 v[128:143], v[184:187], v[120:123], v[128:143]\n"
"  ds_read_b128 v[28:31], v247 offset:3072\n"
"  s_waitcnt lgkmcnt(4)\n"
"  v_mfma_f32_32x32x16_bf16 v[128:143], v[188:191], v[124:127], v[128:143]\n"
"  s_nop 11\n"
"  v_pk_mul_f32 v[136:137], v[136:137], v[246:247] op_sel_hi:[1,0]\n"
"  v_cvt_pk_bf16_f32 v160, v136, v137\n"
"  v_pk_mul_f32 v[138:139], v[138:139], v[246:247] op_sel_hi:[1,0]\n"
"  v_cvt_pk_bf16_f32 v161, v138, v139\n"
"  v_pk_mul_f32 v[140:141], v[140:141], v[246:247] op_sel_hi:[1,0]\n"
"  v_cvt_pk_bf16_f32 v162, v140, v141\n"
"  v_pk_mul_f32 v[142:143], v[142:143], v[246:247] op_sel_hi:[1,0]\n"
"  v_cvt_pk_bf16_f32 v163, v142, v143\n"
"  ds_write_b128 v228, v[160:163]\n"
"  v_pk_mul_f32 v[128:129], v[128:129], v[246:247] op_sel_hi:[1,0]\n"
"  v_cvt_pk_bf16_f32 v196, v128, v129\n"
"  v_pk_mul_f32 v[130:131], v[130:131], v[246:247] op_sel_hi:[1,0]\n"
"  v_cvt_pk_bf16_f32 v197, v130, v131\n"
"  v_pk_mul_f32 v[132:133], v[132:133], v[246:247] op_sel_hi:[1,0]\n"
"  v_cvt_pk_bf16_f32 v198, v132, v133\n"
"  v_pk_mul_f32 v[134:135], v[134:135], v[246:247] op_sel_hi:[1,0]\n"
"  v_cvt_pk_bf16_f32 v199, v134, v135\n"
"  s_waitcnt lgkmcnt(0)\n"
"  s_barrier\n"
"  ds_read_b128 v[80:83], v234\n"
"  ds_read_b128 v[84:87], v201\n"
"  ds_read_b128 v[88:91], v201 offset:1024\n"
"  v_mad_u32_u24 v201, v202, 8, v203\n"
"  s_waitcnt lgkmcnt(2)\n"
"  v_mfma_f32_32x32x16_bf16 v[32:47], v[80:83], v[196:199], v[16:31]\n"
"  ds_read_b128 v[80:83], v233\n"
"  ds_read_b128 v[92:95], v201\n"
"  ds_read_b128 v[96:99], v201 offset:1024\n"
"  s_cmp_eq_u32 s54, 0\n"
"  s_nop 7\n"
"  v_exp_f32 v32, v32\n"
"  v_exp_f32 v33, v33\n"
"  v_exp_f32 v34, v34\n"
"  v_exp_f32 v35, v35\n"
"  v_cvt_pk_bf16_f32 v32, v32, v33\n"
"  v_exp_f32 v36, v36\n"
"  v_exp_f32 v37, v37\n"
"  v_cvt_pk_bf16_f32 v33, v34, v35\n"
"  v_exp_f32 v38, v38\n"
"  v_exp_f32 v39, v39\n"
"  v_cvt_pk_bf16_f32 v34, v36, v37\n"
"  v_exp_f32 v40, v40\n"
"  v_cvt_pk_bf16_f32 v35, v38, v39\n"
"  v_exp_f32 v41, v41\n"
"  v_exp_f32 v42, v42\n"
"  s_waitcnt lgkmcnt(4)\n"
"  v_mfma_f32_32x32x16_bf16 v[64:79], v[84:87], v[32:35], v[64:79]\n"
"  v_exp_f32 v43, v43\n"
"  v_cvt_pk_bf16_f32 v40, v40, v41\n"
"  s_waitcnt lgkmcnt(2)\n"
"  v_mfma_f32_32x32x16_bf16 v[48:63], v[80:83], v[196:199], 0\n"
"  v_exp_f32 v44, v44\n"
"  v_exp_f32 v45, v45\n"
"  v_cvt_pk_bf16_f32 v41, v42, v43\n"
"  v_exp_f32 v46, v46\n"
"  v_exp_f32 v47, v47\n"
"  v_cvt_pk_bf16_f32 v42, v44, v45\n"
"  v_cvt_pk_bf16_f32 v43, v46, v47\n"
"  s_nop 1\n"
"  v_mfma_f32_32x32x16_bf16 v[64:79], v[88:91], v[40:43], v[64:79]\n"
"  s_cbranch_scc1 .Lfb_M_own\n"
"  s_nop 0\n"
"  v_exp_f32 v48, v48\n"
"  v_exp_f32 v49, v49\n"
"  v_exp_f32 v50, v50\n"
"  v_exp_f32 v51, v51\n"
"  v_cvt_pk_bf16_f32 v48, v48, v49\n"
"  v_exp_f32 v52, v52\n"
"  v_exp_f32 v53, v53\n"
"  v_cvt_pk_bf16_f32 v49, v50, v51\n"
"  v_exp_f32 v54, v54\n"
"  v_exp_f32 v55, v55\n"
"  v_cvt_pk_bf16_f32 v50, v52, v53\n"
"  v_exp_f32 v56, v56\n"
"  v_cvt_pk_bf16_f32 v51, v54, v55\n"
"  v_exp_f32 v57, v57\n"
"  v_exp_f32 v58, v58\n"
"  s_waitcnt lgkmcnt(1)\n"
"  v_mfma_f32_32x32x16_bf16 v[64:79], v[92:95], v[48:51], v[64:79]\n"
"  v_exp_f32 v59, v59\n"
"  v_cvt_pk_bf16_f32 v56, v56, v57\n"
"  v_exp_f32 v60, v60\n"
"  v_exp_f32 v61, v61\n"
"  v_cvt_pk_bf16_f32 v57, v58, v59\n"
"  v_exp_f32 v62, v62\n"
"  v_exp_f32 v63, v63\n"
"  v_cvt_pk_bf16_f32 v58, v60, v61\n"
"  v_cvt_pk_bf16_f32 v59, v62, v63\n"
"  s_waitcnt lgkmcnt(0)\n"
"  s_nop 0\n"
"  v_mfma_f32_32x32x16_bf16 v[64:79], v[96:99], v[56:59], v[64:79]\n"
".Lfb_M_own:\n"
"  ds_read_b128 v[180:183], v247 offset:4096\n"
"  ds_read_b128 v[184:187], v247 offset:5120\n"
"  ds_read_b128 v[188:191], v247 offset:6144\n"
"  ds_read_b128 v[192:195], v247 offset:7168\n"
"  v_lshrrev_b32 v249, 4, v255\n"
"  v_lshl_or_b32 v230, s54, 3, v249\n"
"  v_and_b32 v231, 15, v255\n"
"  v_xor_b32 v230, v230, v231\n"
"  v_lshlrev_b32 v230, 4, v230\n"
"  v_lshl_or_b32 v230, v249, 10, v230\n"
"  s_lshl2_add_u32 s69, s52, s45\n"
"  s_lshl_b32 s69, s69, 13\n"
"  s_lshl_b32 s60, s51, 8\n"
"  s_add_u32 s69, s69, s60\n"
"  s_mov_b64 s[4:5], s[32:33]\n"
"  s_mov_b64 s[6:7], s[34:35]\n"
"  v_lshrrev_b32 v249, 3, v255\n"
"  v_and_b32 v232, 0x70, v254\n"
"  v_lshl_or_b32 v232, v249, 10, v232\n"
"  s_sub_u32 s60, s46, 1\n"
"  s_lshl_b32 s60, s60, 5\n"
"  s_add_u32 s60, s60, s47\n"
"  s_lshr_b32 s60, s60, 1\n"
"  s_lshl_b32 s60, s60, 7\n"
"  s_add_u32 s60, s60, s44\n"
"  s_lshl_b32 s73, s60, 7\n"
"  s_add_u32 s74, s73, 8192\n"
"  s_cmp_eq_u32 s3, 0\n"
"  s_cbranch_scc0 .Lfb_M_wdp\n"
"  s_mov_b64 s[4:5], s[24:25]\n"
"  s_mov_b64 s[6:7], s[26:27]\n"
"  s_mov_b64 s[32:33], s[28:29]\n"
"  s_mov_b64 s[34:35], s[30:31]\n"
"  v_lshrrev_b32 v249, 4, v255\n"
"  v_mul_u32_u24 v232, 3584, v249\n"
"  v_bfe_u32 v249, v255, 2, 2\n"
"  v_lshl_add_u32 v232, v249, 7, v232\n"
"  v_and_b32 v249, 0x30, v254\n"
"  v_add_u32 v232, v232, v249\n"
"  s_lshr_b32 s60, s47, 1\n"
"  s_lshl3_add_u32 s60, s60, s44\n"
"  s_mul_i32 s60, s60, 14336\n"
"  s_lshl_b32 s73, s46, 9\n"
"  s_add_u32 s73, s73, s60\n"
"  s_sub_u32 s73, s73, 512\n"
"  s_mov_b32 s74, s73\n"
".Lfb_M_wdp:\n"
"  s_cmp_eq_u32 s46, 0\n"
"  s_cselect_b32 s6, 0, s6\n"
"  s_cselect_b32 s34, 0, s34\n"
"  s_cselect_b32 s73, 0, s73\n"
"  s_cselect_b32 s74, 0, s74\n"
"  v_xor_b32 v231, 64, v230\n"
"  v_add_u32 v231, 3072, v231\n"
"  s_barrier\n"
"  s_cmp_lt_u32 s48, 1\n"
"  s_cbranch_scc1 .Lfb_M_p7\n"
"  ds_read_b128 v[80:83], v200 offset:57344\n"
"  v_sub_u32 v201, v201, v202\n"
"  ds_read_b128 v[84:87], v201\n"
"  ds_read_b128 v[88:91], v201 offset:1024\n"
"  s_waitcnt lgkmcnt(2)\n"
"  v_mfma_f32_32x32x16_bf16 v[32:47], v[80:83], v[196:199], 0\n"
".Lfb_M_p7:\n"
"  ds_read_b64_tr_b16 v[0:1], v206 offset:32768\n"
"  ds_read_b64_tr_b16 v[2:3], v206 offset:33792\n"
"  ds_read_b64_tr_b16 v[4:5], v206 offset:36864\n"
"  ds_read_b64_tr_b16 v[6:7], v206 offset:37888\n"
"  ds_read_b64_tr_b16 v[8:9], v206 offset:40960\n"
"  ds_read_b64_tr_b16 v[10:11], v206 offset:41984\n"
"  ds_read_b64_tr_b16 v[12:13], v206 offset:45056\n"
"  ds_read_b64_tr_b16 v[14:15], v206 offset:46080\n"
"  s_cmp_lt_u32 s48, 1\n"
"  s_cbranch_scc1 .Lfb_M_g7\n"
"  ds_read_b128 v[80:83], v200 offset:49152\n"
"  v_sub_u32 v201, v201, v202\n"
"  ds_read_b128 v[92:95], v201\n"
"  ds_read_b128 v[96:99], v201 offset:1024\n"
"  v_exp_f32 v32, v32\n"
"  v_exp_f32 v33, v33\n"
"  v_exp_f32 v34, v34\n"
"  v_exp_f32 v35, v35\n"
"  v_cvt_pk_bf16_f32 v32, v32, v33\n"
"  v_exp_f32 v36, v36\n"
"  v_exp_f32 v37, v37\n"
"  v_cvt_pk_bf16_f32 v33, v34, v35\n"
"  v_exp_f32 v38, v38\n"
"  v_exp_f32 v39, v39\n"
"  v_cvt_pk_bf16_f32 v34, v36, v37\n"
"  v_exp_f32 v40, v40\n"
"  v_cvt_pk_bf16_f32 v35, v38, v39\n"
"  v_exp_f32 v41, v41\n"
"  v_exp_f32 v42, v42\n"
"  s_waitcnt lgkmcnt(12)\n"
"  v_mfma_f32_32x32x16_bf16 v[64:79], v[84:87], v[32:35], v[64:79]\n"
"  v_exp_f32 v43, v43\n"
"  v_cvt_pk_bf16_f32 v40, v40, v41\n"
"  s_waitcnt lgkmcnt(2)\n"
"  v_mfma_f32_32x32x16_bf16 v[48:63], v[80:83], v[196:199], 0\n"
"  v_exp_f32 v44, v44\n"
"  v_exp_f32 v45, v45\n"
"  v_cvt_pk_bf16_f32 v41, v42, v43\n"
"  v_exp_f32 v46, v46\n"
"  v_exp_f32 v47, v47\n"
"  v_cvt_pk_bf16_f32 v42, v44, v45\n"
"  v_cvt_pk_bf16_f32 v43, v46, v47\n"
"  s_nop 1\n"
"  v_mfma_f32_32x32x16_bf16 v[64:79], v[88:91], v[40:43], v[64:79]\n"
".Lfb_M_g7:\n"
"  s_waitcnt lgkmcnt(0)\n"
"  s_barrier\n"
"  s_add_u32 s60, s89, 1\n"
"  s_and_b32 s60, s60, 7\n"
"  s_lshl_b32 s60, s60, 16\n"
"  s_lshl_b32 s61, s45, 10\n"
"  s_add_u32 s60, s60, s88\n"
"  s_add_u32 m0, s61, 49152\n"
"  s_nop 0\n"
"  buffer_load_dwordx4 v207, s[16:19], s60 offen lds\n"
"  s_add_u32 m0, s61, 53248\n"
"  s_add_u32 s60, s60, 16384\n"
"  buffer_load_dwordx4 v207, s[16:19], s60 offen lds\n"
"  s_add_u32 m0, s61, 57344\n"
"  s_add_u32 s60, s60, 16384\n"
"  buffer_load_dwordx4 v207, s[16:19], s60 offen lds\n"
"  s_add_u32 m0, s61, 61440\n"
"  s_add_u32 s60, s60, 16384\n"
"  buffer_load_dwordx4 v207, s[16:19], s60 offen lds\n"
"  s_lshl_b32 s60, s45, 8\n"
"  s_add_u32 m0, s60, 158720\n"
"  s_nop 0\n"
"  buffer_load_dword v253, s[40:43], s60 offen lds\n"
"  s_cmp_lt_u32 s48, 1\n"
"  s_cbranch_scc1 .Lfb_M_g6\n"
"  ds_read_b128 v[80:83], v200 offset:40960\n"
"  v_sub_u32 v201, v201, v202\n"
"  ds_read_b128 v[84:87], v201\n"
"  ds_read_b128 v[88:91], v201 offset:1024\n"
"  v_exp_f32 v48, v48\n"
"  v_exp_f32 v49, v49\n"
"  v_exp_f32 v50, v50\n"
"  v_exp_f32 v51, v51\n"
"  v_cvt_pk_bf16_f32 v48, v48, v49\n"
"  v_exp_f32 v52, v52\n"
"  v_exp_f32 v53, v53\n"
"  v_cvt_pk_bf16_f32 v49, v50, v51\n"
"  v_exp_f32 v54, v54\n"
"  v_exp_f32 v55, v55\n"
"  v_cvt_pk_bf16_f32 v50, v52, v53\n"
"  v_exp_f32 v56, v56\n"
"  v_cvt_pk_bf16_f32 v51, v54, v55\n"
"  v_exp_f32 v57, v57\n"
"  v_exp_f32 v58, v58\n"
"  v_mfma_f32_32x32x16_bf16 v[64:79], v[92:95], v[48:51], v[64:79]\n"
"  v_exp_f32 v59, v59\n"
"  v_cvt_pk_bf16_f32 v56, v56, v57\n"
"  s_waitcnt lgkmcnt(2)\n"
"  v_mfma_f32_32x32x16_bf16 v[32:47], v[80:83], v[196:199], 0\n"
"  v_exp_f32 v60, v60\n"
"  v_exp_f32 v61, v61\n"
"  v_cvt_pk_bf16_f32 v57, v58, v59\n"
"  v_exp_f32 v62, v62\n"
"  v_exp_f32 v63, v63\n"
"  v_cvt_pk_bf16_f32 v58, v60, v61\n"
"  v_cvt_pk_bf16_f32 v59, v62, v63\n"
"  s_nop 1\n"
"  v_mfma_f32_32x32x16_bf16 v[64:79], v[96:99], v[56:59], v[64:79]\n"
".Lfb_M_g6:\n"
"  s_add_u32 s60, s89, 2\n"
"  s_and_b32 s60, s60, 7\n"
"  s_lshl_b32 s60, s60, 16\n"
"  s_lshl_b32 s61, s45, 10\n"
"  s_add_u32 s60, s60, s88\n"
"  s_add_u32 m0, s61, 0\n"
"  s_nop 0\n"
"  buffer_load_dwordx4 v207, s[16:19], s60 offen lds\n"
"  s_add_u32 m0, s61, 4096\n"
"  s_add_u32 s60, s60, 16384\n"
"  buffer_load_dwordx4 v207, s[16:19], s60 offen lds\n"
"  s_add_u32 m0, s61, 8192\n"
"  s_add_u32 s60, s60, 16384\n"
"  buffer_load_dwordx4 v207, s[16:19], s60 offen lds\n"
"  s_add_u32 m0, s61, 12288\n"
"  s_add_u32 s60, s60, 16384\n"
"  buffer_load_dwordx4 v207, s[16:19], s60 offen lds\n"
"  s_cmp_lt_u32 s48, 2\n"
"  s_cbranch_scc1 .Lfb_M_g5\n"
"  ds_read_b128 v[80:83], v200 offset:32768\n"
"  v_sub_u32 v201, v201, v202\n"
"  ds_read_b128 v[92:95], v201\n"
"  ds_read_b128 v[96:99], v201 offset:1024\n"
"  v_exp_f32 v32, v32\n"
"  v_exp_f32 v33, v33\n"
"  v_exp_f32 v34, v34\n"
"  v_exp_f32 v35, v35\n"
"  v_cvt_pk_bf16_f32 v32, v32, v33\n"
"  v_exp_f32 v36, v36\n"
"  v_exp_f32 v37, v37\n"
"  v_cvt_pk_bf16_f32 v33, v34, v35\n"
"  v_exp_f32 v38, v38\n"
"  v_exp_f32 v39, v39\n"
"  v_cvt_pk_bf16_f32 v34, v36, v37\n"
"  v_exp_f32 v40, v40\n"
"  v_cvt_pk_bf16_f32 v35, v38, v39\n"
"  v_exp_f32 v41, v41\n"
"  v_exp_f32 v42, v42\n"
"  s_waitcnt lgkmcnt(4)\n"
"  v_mfma_f32_32x32x16_bf16 v[64:79], v[84:87], v[32:35], v[64:79]\n"
"  v_exp_f32 v43, v43\n"
"  v_cvt_pk_bf16_f32 v40, v40, v41\n"
"  s_waitcnt lgkmcnt(2)\n"
"  v_mfma_f32_32x32x16_bf16 v[48:63], v[80:83], v[196:199], 0\n"
"  v_exp_f32 v44, v44\n"
"  v_exp_f32 v45, v45\n"
"  v_cvt_pk_bf16_f32 v41, v42, v43\n"
"  v_exp_f32 v46, v46\n"
"  v_exp_f32 v47, v47\n"
"  v_cvt_pk_bf16_f32 v42, v44, v45\n"
"  v_cvt_pk_bf16_f32 v43, v46, v47\n"
"  s_nop 1\n"
"  v_mfma_f32_32x32x16_bf16 v[64:79], v[88:91], v[40:43], v[64:79]\n"
".Lfb_M_g5:\n"
"  s_add_u32 s60, s89, 3\n"
"  s_and_b32 s60, s60, 7\n"
"  s_lshl_b32 s60, s60, 16\n"
"  s_lshl_b32 s61, s45, 10\n"
"  s_add_u32 s60, s60, s88\n"
"  s_add_u32 m0, s61, 16384\n"
"  s_nop 0\n"
"  buffer_load_dwordx4 v207, s[16:19], s60 offen lds\n"
"  s_add_u32 m0, s61, 20480\n"
"  s_add_u32 s60, s60, 16384\n"
"  buffer_load_dwordx4 v207, s[16:19], s60 offen lds\n"
"  s_add_u32 m0, s61, 24576\n"
"  s_add_u32 s60, s60, 16384\n"
"  buffer_load_dwordx4 v207, s[16:19], s60 offen lds\n"
"  s_add_u32 m0, s61, 28672\n"
"  s_add_u32 s60, s60, 16384\n"
"  buffer_load_dwordx4 v207, s[16:19], s60 offen lds\n"
"  s_barrier\n"
"  s_add_u32 s60, s89, 4\n"
"  s_and_b32 s60, s60, 7\n"
"  s_lshl_b32 s60, s60, 16\n"
"  s_lshl_b32 s61, s45, 10\n"
"  s_add_u32 s60, s60, s88\n"
"  s_add_u32 m0, s61, 32768\n"
"  s_nop 0\n"
"  buffer_load_dwordx4 v207, s[16:19], s60 offen lds\n"
"  s_add_u32 m0, s61, 36864\n"
"  s_add_u32 s60, s60, 16384\n"
"  buffer_load_dwordx4 v207, s[16:19], s60 offen lds\n"
"  s_add_u32 m0, s61, 40960\n"
"  s_add_u32 s60, s60, 16384\n"
"  buffer_load_dwordx4 v207, s[16:19], s60 offen lds\n"
"  s_add_u32 m0, s61, 45056\n"
"  s_add_u32 s60, s60, 16384\n"
"  buffer_load_dwordx4 v207, s[16:19], s60 offen lds\n"
"  s_lshl_b32 s60, s45, 11\n"
"  s_add_u32 m0, s60, 65536\n"
"  s_nop 0\n"
"  buffer_load_dwordx4 v230, s[8:11], s69 offen lds\n"
"  buffer_load_dwordx4 v231, s[8:11], s69 offen offset:1024 lds\n"
"  s_cmp_lt_u32 s48, 2\n"
"  s_cbranch_scc1 .Lfb_M_g4\n"
"  ds_read_b128 v[80:83], v200 offset:24576\n"
"  v_sub_u32 v201, v201, v202\n"
"  ds_read_b128 v[84:87], v201\n"
"  ds_read_b128 v[88:91], v201 offset:1024\n"
"  v_exp_f32 v48, v48\n"
"  v_exp_f32 v49, v49\n"
"  v_exp_f32 v50, v50\n"
"  v_exp_f32 v51, v51\n"
"  v_cvt_pk_bf16_f32 v48, v48, v49\n"
"  v_exp_f32 v52, v52\n"
"  v_exp_f32 v53, v53\n"
"  v_cvt_pk_bf16_f32 v49, v50, v51\n"
"  v_exp_f32 v54, v54\n"
"  v_exp_f32 v55, v55\n"
"  v_cvt_pk_bf16_f32 v50, v52, v53\n"
"  v_exp_f32 v56, v56\n"
"  v_cvt_pk_bf16_f32 v51, v54, v55\n"
"  v_exp_f32 v57, v57\n"
"  v_exp_f32 v58, v58\n"
"  s_waitcnt lgkmcnt(4)\n"
"  v_mfma_f32_32x32x16_bf16 v[64:79], v[92:95], v[48:51], v[64:79]\n"
"  v_exp_f32 v59, v59\n"
"  v_cvt_pk_bf16_f32 v56, v56, v57\n"
"  s_waitcnt lgkmcnt(2)\n"
"  v_mfma_f32_32x32x16_bf16 v[32:47], v[80:83], v[196:199], 0\n"
"  v_exp_f32 v60, v60\n"
"  v_exp_f32 v61, v61\n"
"  v_cvt_pk_bf16_f32 v57, v58, v59\n"
"  v_exp_f32 v62, v62\n"
"  v_exp_f32 v63, v63\n"
"  v_cvt_pk_bf16_f32 v58, v60, v61\n"
"  v_cvt_pk_bf16_f32 v59, v62, v63\n"
"  s_nop 1\n"
"  v_mfma_f32_32x32x16_bf16 v[64:79], v[96:99], v[56:59], v[64:79]\n"
".Lfb_M_g4:\n"
"  buffer_load_dwordx4 v[212:215], v232, s[4:7], s73 offen\n"
"  buffer_load_dwordx4 v[216:219], v232, s[32:35], s74 offen\n"
"  s_cmp_lt_u32 s48, 3\n"
"  s_cbranch_scc1 .Lfb_M_g2\n"
"  ds_read_b128 v[80:83], v200 offset:16384\n"
"  v_sub_u32 v201, v201, v202\n"
"  ds_read_b128 v[92:95], v201\n"
"  ds_read_b128 v[96:99], v201 offset:1024\n"
"  v_exp_f32 v32, v32\n"
"  v_exp_f32 v33, v33\n"
"  v_exp_f32 v34, v34\n"
"  v_exp_f32 v35, v35\n"
"  v_cvt_pk_bf16_f32 v32, v32, v33\n"
"  v_exp_f32 v36, v36\n"
"  v_exp_f32 v37, v37\n"
"  v_cvt_pk_bf16_f32 v33, v34, v35\n"
"  v_exp_f32 v38, v38\n"
"  v_exp_f32 v39, v39\n"
"  v_cvt_pk_bf16_f32 v34, v36, v37\n"
"  v_exp_f32 v40, v40\n"
"  v_cvt_pk_bf16_f32 v35, v38, v39\n"
"  v_exp_f32 v41, v41\n"
"  v_exp_f32 v42, v42\n"
"  s_waitcnt lgkmcnt(4)\n"
"  v_mfma_f32_32x32x16_bf16 v[64:79], v[84:87], v[32:35], v[64:79]\n"
"  v_exp_f32 v43, v43\n"
"  v_cvt_pk_bf16_f32 v40, v40, v41\n"
"  s_waitcnt lgkmcnt(2)\n"
"  v_mfma_f32_32x32x16_bf16 v[48:63], v[80:83], v[196:199], 0\n"
"  v_exp_f32 v44, v44\n"
"  v_exp_f32 v45, v45\n"
"  v_cvt_pk_bf16_f32 v41, v42, v43\n"
"  v_exp_f32 v46, v46\n"
"  v_exp_f32 v47, v47\n"
"  v_cvt_pk_bf16_f32 v42, v44, v45\n"
"  v_cvt_pk_bf16_f32 v43, v46, v47\n"
"  s_nop 1\n"
"  v_mfma_f32_32x32x16_bf16 v[64:79], v[88:91], v[40:43], v[64:79]\n"
"  ds_read_b128 v[80:83], v200 offset:8192\n"
"  v_sub_u32 v201, v201, v202\n"
"  ds_read_b128 v[84:87], v201\n"
"  ds_read_b128 v[88:91], v201 offset:1024\n"
"  v_exp_f32 v48, v48\n"
"  v_exp_f32 v49, v49\n"
"  v_exp_f32 v50, v50\n"
"  v_exp_f32 v51, v51\n"
"  v_cvt_pk_bf16_f32 v48, v48, v49\n"
"  v_exp_f32 v52, v52\n"
"  v_exp_f32 v53, v53\n"
"  v_cvt_pk_bf16_f32 v49, v50, v51\n"
"  v_exp_f32 v54, v54\n"
"  v_exp_f32 v55, v55\n"
"  v_cvt_pk_bf16_f32 v50, v52, v53\n"
"  v_exp_f32 v56, v56\n"
"  v_cvt_pk_bf16_f32 v51, v54, v55\n"
"  v_exp_f32 v57, v57\n"
"  v_exp_f32 v58, v58\n"
"  s_waitcnt lgkmcnt(4)\n"
"  v_mfma_f32_32x32x16_bf16 v[64:79], v[92:95], v[48:51], v[64:79]\n"
"  v_exp_f32 v59, v59\n"
"  v_cvt_pk_bf16_f32 v56, v56, v57\n"
"  s_cmp_eq_u32 s54, 0\n"
"  s_cbranch_scc1 .Lfb_M_s2a\n"
"  s_waitcnt lgkmcnt(2)\n"
"  v_mfma_f32_32x32x16_bf16 v[32:47], v[80:83], v[196:199], v[180:195]\n"
"  s_branch .Lfb_M_s2b\n"
".Lfb_M_s2a:\n"
"  s_waitcnt lgkmcnt(2)\n"
"  v_mfma_f32_32x32x16_bf16 v[32:47], v[80:83], v[196:199], 0\n"
".Lfb_M_s2b:\n"
"  v_exp_f32 v60, v60\n"
"  v_exp_f32 v61, v61\n"
"  v_cvt_pk_bf16_f32 v57, v58, v59\n"
"  v_exp_f32 v62, v62\n"
"  v_exp_f32 v63, v63\n"
"  v_cvt_pk_bf16_f32 v58, v60, v61\n"
"  v_cvt_pk_bf16_f32 v59, v62, v63\n"
"  s_nop 1\n"
"  v_mfma_f32_32x32x16_bf16 v[64:79], v[96:99], v[56:59], v[64:79]\n"
".Lfb_M_g2:\n"
"  s_cmp_lt_u32 s48, 4\n"
"  s_cbranch_scc1 .Lfb_M_g1\n"
"  ds_read_b128 v[80:83], v200\n"
"  v_sub_u32 v201, v201, v202\n"
"  ds_read_b128 v[92:95], v201\n"
"  ds_read_b128 v[96:99], v201 offset:1024\n"
"  v_exp_f32 v32, v32\n"
"  v_exp_f32 v33, v33\n"
"  v_exp_f32 v34, v34\n"
"  v_exp_f32 v35, v35\n"
"  v_cvt_pk_bf16_f32 v32, v32, v33\n"
"  v_exp_f32 v36, v36\n"
"  v_exp_f32 v37, v37\n"
"  v_cvt_pk_bf16_f32 v33, v34, v35\n"
"  v_exp_f32 v38, v38\n"
"  v_exp_f32 v39, v39\n"
"  v_cvt_pk_bf16_f32 v34, v36, v37\n"
"  v_exp_f32 v40, v40\n"
"  v_cvt_pk_bf16_f32 v35, v38, v39\n"
"  v_exp_f32 v41, v41\n"
"  v_exp_f32 v42, v42\n"
"  s_waitcnt lgkmcnt(4)\n"
"  v_mfma_f32_32x32x16_bf16 v[64:79], v[84:87], v[32:35], v[64:79]\n"
"  v_exp_f32 v43, v43\n"
"  v_cvt_pk_bf16_f32 v40, v40, v41\n"
"  s_waitcnt lgkmcnt(2)\n"
"  v_mfma_f32_32x32x16_bf16 v[48:63], v[80:83], v[196:199], v[180:195]\n"
"  v_exp_f32 v44, v44\n"
"  v_exp_f32 v45, v45\n"
"  v_cvt_pk_bf16_f32 v41, v42, v43\n"
"  v_exp_f32 v46, v46\n"
"  v_exp_f32 v47, v47\n"
"  v_cvt_pk_bf16_f32 v42, v44, v45\n"
"  v_cvt_pk_bf16_f32 v43, v46, v47\n"
"  s_nop 1\n"
"  v_mfma_f32_32x32x16_bf16 v[64:79], v[88:91], v[40:43], v[64:79]\n"
"  s_cmp_eq_u32 s54, 1\n"
"  s_cbranch_scc1 .Lfb_M_g1\n"
"  v_exp_f32 v48, v48\n"
"  v_exp_f32 v49, v49\n"
"  v_exp_f32 v50, v50\n"
"  v_exp_f32 v51, v51\n"
"  v_cvt_pk_bf16_f32 v48, v48, v49\n"
"  v_exp_f32 v52, v52\n"
"  v_exp_f32 v53, v53\n"
"  v_cvt_pk_bf16_f32 v49, v50, v51\n"
"  v_exp_f32 v54, v54\n"
"  v_exp_f32 v55, v55\n"
"  v_cvt_pk_bf16_f32 v50, v52, v53\n"
"  v_exp_f32 v56, v56\n"
"  v_cvt_pk_bf16_f32 v51, v54, v55\n"
"  v_exp_f32 v57, v57\n"
"  v_exp_f32 v58, v58\n"
"  s_waitcnt lgkmcnt(1)\n"
"  v_mfma_f32_32x32x16_bf16 v[64:79], v[92:95], v[48:51], v[64:79]\n"
"  v_exp_f32 v59, v59\n"
"  v_cvt_pk_bf16_f32 v56, v56, v57\n"
"  v_exp_f32 v60, v60\n"
"  v_exp_f32 v61, v61\n"
"  v_cvt_pk_bf16_f32 v57, v58, v59\n"
"  v_exp_f32 v62, v62\n"
"  v_exp_f32 v63, v63\n"
"  v_cvt_pk_bf16_f32 v58, v60, v61\n"
"  v_cvt_pk_bf16_f32 v59, v62, v63\n"
"  s_waitcnt lgkmcnt(0)\n"
"  s_nop 0\n"
"  v_mfma_f32_32x32x16_bf16 v[64:79], v[96:99], v[56:59], v[64:79]\n"
".Lfb_M_g1:\n"
"  s_lshl1_add_u32 s60, s48, s54\n"
"  s_lshl3_add_u32 s60, s60, s49\n"
"  s_lshl1_add_u32 s60, s60, s55\n"
"  s_mul_i32 s60, s60, 2560\n"
"  s_add_u32 s60, s60, 6291456\n"
"  s_mov_b32 s61, 0x21800000\n"
"  s_mov_b32 s62, 0x5d800000\n"
"  s_nop 4\n"
"  v_cmp_nlt_f32 vcc, s61, v72\n"
"  v_cmp_ngt_f32 s[90:91], s62, v72\n"
"  s_nop 0\n"
"  s_or_b64 vcc, vcc, s[90:91]\n"
"  s_cbranch_vccnz .Lfb_slow\n"
".Lfb_M_norm:\n"
"  s_nop 4\n"
"  v_rcp_f32 v72, v72\n"
"  s_nop 0\n"
"  v_pk_mul_f32 v[64:65], v[64:65], v[72:73] op_sel_hi:[1,0]\n"
"  v_pk_mul_f32 v[66:67], v[66:67], v[72:73] op_sel_hi:[1,0]\n"
"  v_pk_mul_f32 v[68:69], v[68:69], v[72:73] op_sel_hi:[1,0]\n"
"  v_pk_mul_f32 v[70:71], v[70:71], v[72:73] op_sel_hi:[1,0]\n"
"  v_cvt_pk_bf16_f32 v64, v64, v65\n"
"  v_cvt_pk_bf16_f32 v65, v66, v67\n"
"  v_cvt_pk_bf16_f32 v66, v68, v69\n"
"  v_cvt_pk_bf16_f32 v67, v70, v71\n"
"  buffer_store_dwordx4 v[64:67], v254, s[20:23], s60 offen sc1\n"
"  v_add3_u32 v73, v64, v65, s56\n"
"  v_add3_u32 v73, v73, v66, v67\n"
"  buffer_store_dword v73, v253, s[20:23], s60 offen offset:1024 sc1\n"
"  s_waitcnt vmcnt(4)\n"
"  s_waitcnt lgkmcnt(0)\n"
"  s_barrier\n"
"  ds_read_b64_tr_b16 v[16:17], v206 offset:49152\n"
"  ds_read_b64_tr_b16 v[18:19], v206 offset:50176\n"
"  ds_read_b64_tr_b16 v[20:21], v206 offset:53248\n"
"  ds_read_b64_tr_b16 v[22:23], v206 offset:54272\n"
"  ds_read_b64_tr_b16 v[24:25], v206 offset:57344\n"
"  ds_read_b64_tr_b16 v[26:27], v206 offset:58368\n"
"  ds_read_b64_tr_b16 v[28:29], v206 offset:61440\n"
"  ds_read_b64_tr_b16 v[30:31], v206 offset:62464\n"
"  ds_read_b64_tr_b16 v[32:33], v206\n"
"  ds_read_b64_tr_b16 v[34:35], v206 offset:1024\n"
"  ds_read_b64_tr_b16 v[36:37], v206 offset:4096\n"
"  ds_read_b64_tr_b16 v[38:39], v206 offset:5120\n"
"  ds_read_b64_tr_b16 v[40:41], v206 offset:8192\n"
"  ds_read_b64_tr_b16 v[42:43], v206 offset:9216\n"
"  ds_read_b64_tr_b16 v[44:45], v206 offset:12288\n"
"  ds_read_b64_tr_b16 v[46:47], v206 offset:13312\n"
"  ds_read_b64_tr_b16 v[48:49], v206 offset:16384\n"
"  ds_read_b64_tr_b16 v[50:51], v206 offset:17408\n"
"  ds_read_b64_tr_b16 v[52:53], v206 offset:20480\n"
"  ds_read_b64_tr_b16 v[54:55], v206 offset:21504\n"
"  ds_read_b64_tr_b16 v[56:57], v206 offset:24576\n"
"  ds_read_b64_tr_b16 v[58:59], v206 offset:25600\n"
"  ds_read_b64_tr_b16 v[60:61], v206 offset:28672\n"
"  ds_read_b64_tr_b16 v[62:63], v206 offset:29696\n"
"  ds_read_b64_tr_b16 v[64:65], v206 offset:32768\n"
"  ds_read_b64_tr_b16 v[66:67], v206 offset:33792\n"
"  ds_read_b64_tr_b16 v[68:69], v206 offset:36864\n"
"  ds_read_b64_tr_b16 v[70:71], v206 offset:37888\n"
"  ds_read_b64_tr_b16 v[72:73], v206 offset:40960\n"
"  ds_read_b64_tr_b16 v[74:75], v206 offset:41984\n"
"  ds_read_b64_tr_b16 v[76:77], v206 offset:45056\n"
"  ds_read_b64_tr_b16 v[78:79], v206 offset:46080\n"
"  v_and_b32 v96, 31, v255\n"
"  v_lshrrev_b32 v97, 5, v255\n"
"  v_and_b32 v98, 15, v255\n"
"  s_lshl_b32 s60, s45, 2\n"
"  v_xor_b32 v98, s60, v98\n"
"  v_lshlrev_b32 v98, 4, v98\n"
"  v_lshl_or_b32 v98, v96, 8, v98\n"
"  v_lshl_or_b32 v98, v97, 3, v98\n"
"  v_add_u32 v98, 65536, v98\n"
"  s_lshl2_add_u32 s60, s51, s45\n"
"  s_lshl_b32 s60, s60, 6\n"
"  s_add_u32 s60, s60, 158720\n"
"  v_lshl_add_u32 v99, v97, 3, s60\n"
"  ds_read_b64 v[104:105], v98\n"
"  ds_read_b64 v[112:113], v99\n"
"  v_xor_b32 v100, 16, v98\n"
"  ds_read_b64 v[106:107], v100\n"
"  ds_read_b64 v[114:115], v99 offset:16\n"
"  v_xor_b32 v100, 32, v98\n"
"  ds_read_b64 v[108:109], v100\n"
"  ds_read_b64 v[116:117], v99 offset:32\n"
"  v_xor_b32 v100, 48, v98\n"
"  ds_read_b64 v[110:111], v100\n"
"  ds_read_b64 v[118:119], v99 offset:48\n"
"  v_add_u32 v234, 73728, v254\n"
"  v_add_u32 v235, 106496, v206\n"
"  s_lshl_b32 s60, s45, 10\n"
"  s_add_u32 s60, s60, 65536\n"
"  v_add_u32 v236, s60, v254\n"
"  v_mul_u32_u24 v237, 528, v96\n"
"  v_lshl_add_u32 v237, v97, 4, v237\n"
"  s_lshl_b32 s60, s45, 7\n"
"  s_add_u32 s60, s60, 73728\n"
"  v_add_u32 v237, s60, v237\n"
"  s_waitcnt lgkmcnt(7)\n"
"  v_lshlrev_b32 v128, 16, v104\n"
"  v_and_b32 v129, s59, v104\n"
"  s_waitcnt lgkmcnt(6)\n"
"  v_lshlrev_b32 v144, 16, v112\n"
"  v_and_b32 v145, s59, v112\n"
"  v_lshlrev_b32 v130, 16, v105\n"
"  v_and_b32 v131, s59, v105\n"
"  v_lshlrev_b32 v146, 16, v113\n"
"  v_and_b32 v147, s59, v113\n"
"  s_waitcnt lgkmcnt(5)\n"
"  v_lshlrev_b32 v132, 16, v106\n"
"  v_and_b32 v133, s59, v106\n"
"  s_waitcnt lgkmcnt(4)\n"
"  v_lshlrev_b32 v148, 16, v114\n"
"  v_and_b32 v149, s59, v114\n"
"  v_lshlrev_b32 v134, 16, v107\n"
"  v_and_b32 v135, s59, v107\n"
"  v_lshlrev_b32 v150, 16, v115\n"
"  v_and_b32 v151, s59, v115\n"
"  s_waitcnt lgkmcnt(3)\n"
"  v_lshlrev_b32 v136, 16, v108\n"
"  v_and_b32 v137, s59, v108\n"
"  s_waitcnt lgkmcnt(2)\n"
"  v_lshlrev_b32 v152, 16, v116\n"
"  v_and_b32 v153, s59, v116\n"
"  v_lshlrev_b32 v138, 16, v109\n"
"  v_and_b32 v139, s59, v109\n"
"  v_lshlrev_b32 v154, 16, v117\n"
"  v_and_b32 v155, s59, v117\n"
"  s_waitcnt lgkmcnt(1)\n"
"  v_lshlrev_b32 v140, 16, v110\n"
"  v_and_b32 v141, s59, v110\n"
"  s_waitcnt lgkmcnt(0)\n"
"  v_lshlrev_b32 v156, 16, v118\n"
"  v_and_b32 v157, s59, v118\n"
"  v_lshlrev_b32 v142, 16, v111\n"
"  v_and_b32 v143, s59, v111\n"
"  v_lshlrev_b32 v158, 16, v119\n"
"  v_and_b32 v159, s59, v119\n"
"  v_add_u32 v120, 158720, v254\n"
"  ds_read_b128 v[120:123], v120\n"
"  s_waitcnt lgkmcnt(0)\n"
"  v_lshlrev_b32 v208, 16, v120\n"
"  v_and_b32 v124, s59, v120\n"
"  v_add_f32 v208, v208, v124\n"
"  v_lshlrev_b32 v124, 16, v121\n"
"  v_add_f32 v208, v208, v124\n"
"  v_and_b32 v124, s59, v121\n"
"  v_add_f32 v208, v208, v124\n"
"  v_lshlrev_b32 v124, 16, v122\n"
"  v_add_f32 v208, v208, v124\n"
"  v_and_b32 v124, s59, v122\n"
"  v_add_f32 v208, v208, v124\n"
"  v_lshlrev_b32 v124, 16, v123\n"
"  v_add_f32 v208, v208, v124\n"
"  v_and_b32 v124, s59, v123\n"
"  v_add_f32 v208, v208, v124\n"
"  s_nop 1\n"
"  v_add_f32_dpp v208, v208, v208 quad_perm:[1,0,3,2] row_mask:0xf bank_mask:0xf\n"
"  s_nop 1\n"
"  v_add_f32_dpp v208, v208, v208 quad_perm:[2,3,0,1] row_mask:0xf bank_mask:0xf\n"
"  s_nop 1\n"
"  v_add_f32_dpp v208, v208, v208 row_half_mirror row_mask:0xf bank_mask:0xf\n"
"  s_nop 1\n"
"  v_add_f32_dpp v208, v208, v208 row_mirror row_mask:0xf bank_mask:0xf\n"
"  v_mov_b32 v120, v208\n"
"  s_nop 1\n"
"  v_permlane16_swap_b32 v208, v120\n"
"  v_add_f32 v208, v208, v120\n"
"  v_mov_b32 v120, v208\n"
"  s_nop 1\n"
"  v_permlane32_swap_b32 v208, v120\n"
"  v_add_f32 v208, v208, v120\n"
"  s_add_u32 s60, s50, s44\n"
"  s_and_b32 s60, s60, 7\n"
"  s_lshl3_add_u32 s60, s46, s60\n"
"  s_lshl_b32 s80, s60, 11\n"
"  s_add_u32 s80, s80, 65536\n"
"  s_lshl_b32 s60, s60, 5\n"
"  s_sub_u32 s60, 2047, s60\n"
"  v_and_b32 v227, 31, v255\n"
"  v_sub_u32 v227, s60, v227\n"
"  s_lshl2_add_u32 s60, s44, 155904\n"
"  v_mov_b32 v228, s60\n"
"  s_mov_b32 s81, 0x3b000000\n"
"  s_mov_b32 s82, 0xfffff800\n"
"  v_lshlrev_b32 v238, 3, v96\n"
"  v_add_u32 v238, 156160, v238\n"
"  v_and_b32 v98, 15, v255\n"
"  v_lshlrev_b32 v239, 5, v98\n"
"  v_add_u32 v239, 73728, v239\n"
"  v_lshrrev_b32 v99, 4, v255\n"
"  v_max_u32 v100, 1, v99\n"
"  v_mul_u32_u24 v100, 12288, v100\n"
"  v_lshl_add_u32 v242, v98, 4, v100\n"
"  v_lshl_add_u32 v243, v98, 3, v100\n"
"  v_add_u32 v100, 4, v99\n"
"  v_mul_u32_u24 v100, 12288, v100\n"
"  v_lshl_add_u32 v240, v98, 4, v100\n"
"  v_lshl_add_u32 v241, v98, 3, v100\n"
"  s_lshl2_add_u32 s83, s46, s51\n"
"  s_mul_i32 s83, s83, 384\n"
"  s_add_u32 s83, s83, 196608\n"
"  v_lshl_or_b32 v99, s45, 2, v99\n"
"  v_mul_u32_u24 v100, 528, v99\n"
"  v_add_u32 v244, v239, v100\n"
"  v_lshlrev_b32 v245, 3, v99\n"
"  v_add_u32 v245, 156160, v245\n"
"  v_lshl_or_b32 v247, s52, 5, v99\n"
"  v_lshlrev_b32 v246, 4, v98\n"
"  v_lshl_or_b32 v246, v247, 10, v246\n"
"  v_lshl_or_b32 v246, s51, 8, v246\n"
"  v_mov_b32 v229, 155904\n"
"  s_waitcnt lgkmcnt(0)\n"
"  s_barrier\n"
"  ds_read_b128 v[160:163], v234\n"
"  ds_read_b128 v[164:167], v234 offset:1024\n"
"  ds_read_b128 v[168:171], v234 offset:2048\n"
"  ds_read_b128 v[172:175], v234 offset:3072\n"
"  ds_read_b128 v[176:179], v234 offset:4096\n"
"  ds_read_b64_tr_b16 v[80:81], v235\n"
"  ds_read_b64_tr_b16 v[82:83], v235 offset:1024\n"
"  s_waitcnt lgkmcnt(6)\n"
"  v_mfma_f32_32x32x16_bf16 v[128:143], v[0:3], v[160:163], v[128:143]\n"
"  ds_read_b128 v[160:163], v234 offset:5120\n"
"  ds_read_b64_tr_b16 v[84:85], v235 offset:4096\n"
"  ds_read_b64_tr_b16 v[86:87], v235 offset:5120\n"
"  s_waitcnt lgkmcnt(8)\n"
"  v_mfma_f32_32x32x16_bf16 v[128:143], v[4:7], v[164:167], v[128:143]\n"
"  ds_read_b128 v[164:167], v234 offset:6144\n"
"  ds_read_b64_tr_b16 v[88:89], v235 offset:8192\n"
"  ds_read_b64_tr_b16 v[90:91], v235 offset:9216\n"
"  s_waitcnt lgkmcnt(10)\n"
"  v_mfma_f32_32x32x16_bf16 v[128:143], v[8:11], v[168:171], v[128:143]\n"
"  ds_read_b128 v[168:171], v234 offset:7168\n"
"  ds_read_b64_tr_b16 v[92:93], v235 offset:12288\n"
"  ds_read_b64_tr_b16 v[94:95], v235 offset:13312\n"
"  s_waitcnt lgkmcnt(12)\n"
"  v_mfma_f32_32x32x16_bf16 v[128:143], v[12:15], v[172:175], v[128:143]\n"
"  s_barrier\n"
"  ds_read_b128 v[172:175], v234 offset:8192\n"
"  ds_read_b64_tr_b16 v[96:97], v235 offset:16384\n"
"  ds_read_b64_tr_b16 v[98:99], v235 offset:17408\n"
"  s_waitcnt lgkmcnt(14)\n"
"  v_mfma_f32_32x32x16_bf16 v[128:143], v[16:19], v[176:179], v[128:143]\n"
"  ds_read_b128 v[176:179], v234 offset:9216\n"
"  ds_read_b64_tr_b16 v[100:101], v235 offset:20480\n"
"  ds_read_b64_tr_b16 v[102:103], v235 offset:21504\n"
"  s_waitcnt lgkmcnt(14)\n"
"  v_mfma_f32_32x32x16_bf16 v[128:143], v[20:23], v[160:163], v[128:143]\n"
"  ds_read_b128 v[160:163], v234 offset:10240\n"
"  ds_read_b64_tr_b16 v[104:105], v235 offset:24576\n"
"  ds_read_b64_tr_b16 v[106:107], v235 offset:25600\n"
"  s_waitcnt lgkmcnt(14)\n"
"  v_mfma_f32_32x32x16_bf16 v[128:143], v[24:27], v[164:167], v[128:143]\n"
"  ds_read_b128 v[164:167], v234 offset:11264\n"
"  ds_read_b64_tr_b16 v[108:109], v235 offset:28672\n"
"  ds_read_b64_tr_b16 v[110:111], v235 offset:29696\n"
"  s_waitcnt lgkmcnt(14)\n"
"  v_mfma_f32_32x32x16_bf16 v[128:143], v[28:31], v[168:171], v[128:143]\n"
"  ds_read_b128 v[168:171], v234 offset:12288\n"
"  ds_read_b64_tr_b16 v[112:113], v235 offset:32768\n"
"  ds_read_b64_tr_b16 v[114:115], v235 offset:33792\n"
"  s_waitcnt lgkmcnt(14)\n"
"  v_mfma_f32_32x32x16_bf16 v[128:143], v[32:35], v[172:175], v[128:143]\n"
"  ds_read_b128 v[172:175], v234 offset:13312\n"
"  ds_read_b64_tr_b16 v[116:117], v235 offset:36864\n"
"  ds_read_b64_tr_b16 v[118:119], v235 offset:37888\n"
"  s_waitcnt lgkmcnt(14)\n"
"  v_mfma_f32_32x32x16_bf16 v[128:143], v[36:39], v[176:179], v[128:143]\n"
"  ds_read_b128 v[176:179], v234 offset:14336\n"
"  ds_read_b64_tr_b16 v[120:121], v235 offset:40960\n"
"  ds_read_b64_tr_b16 v[122:123], v235 offset:41984\n"
"  s_waitcnt lgkmcnt(14)\n"
"  v_mfma_f32_32x32x16_bf16 v[128:143], v[40:43], v[160:163], v[128:143]\n"
"  ds_read_b128 v[160:163], v234 offset:15360\n"
"  ds_read_b64_tr_b16 v[124:125], v235 offset:45056\n"
"  ds_read_b64_tr_b16 v[126:127], v235 offset:46080\n"
"  s_waitcnt lgkmcnt(14)\n"
"  v_mfma_f32_32x32x16_bf16 v[128:143], v[44:47], v[164:167], v[128:143]\n"
"  s_barrier\n"
"  ds_read_b128 v[164:167], v234 offset:16384\n"
"  s_waitcnt lgkmcnt(12)\n"
"  v_mfma_f32_32x32x16_bf16 v[128:143], v[48:51], v[168:171], v[128:143]\n"
"  ds_read_b128 v[168:171], v234 offset:17408\n"
"  s_waitcnt lgkmcnt(10)\n"
"  v_mfma_f32_32x32x16_bf16 v[128:143], v[52:55], v[172:175], v[128:143]\n"
"  ds_read_b128 v[172:175], v234 offset:18432\n"
"  s_waitcnt lgkmcnt(8)\n"
"  v_mfma_f32_32x32x16_bf16 v[128:143], v[56:59], v[176:179], v[128:143]\n"
"  ds_read_b128 v[176:179], v234 offset:19456\n"
"  s_waitcnt lgkmcnt(6)\n"
"  v_mfma_f32_32x32x16_bf16 v[128:143], v[60:63], v[160:163], v[128:143]\n"
"  ds_read_b128 v[160:163], v234 offset:20480\n"
"  s_waitcnt lgkmcnt(4)\n"
"  v_mfma_f32_32x32x16_bf16 v[128:143], v[64:67], v[164:167], v[128:143]\n"
"  ds_read_b128 v[164:167], v234 offset:21504\n"
"  s_waitcnt lgkmcnt(4)\n"
"  v_mfma_f32_32x32x16_bf16 v[128:143], v[68:71], v[168:171], v[128:143]\n"
"  ds_read_b128 v[168:171], v234 offset:22528\n"
"  s_waitcnt lgkmcnt(4)\n"
"  v_mfma_f32_32x32x16_bf16 v[128:143], v[72:75], v[172:175], v[128:143]\n"
"  ds_read_b128 v[172:175], v234 offset:23552\n"
"  s_waitcnt lgkmcnt(4)\n"
"  v_mfma_f32_32x32x16_bf16 v[128:143], v[76:79], v[176:179], v[128:143]\n"
"  s_barrier\n"
"  ds_read_b128 v[176:179], v234 offset:24576\n"
"  s_waitcnt lgkmcnt(4)\n"
"  v_mfma_f32_32x32x16_bf16 v[128:143], v[80:83], v[160:163], v[128:143]\n"
"  ds_read_b128 v[160:163], v234 offset:25600\n"
"  s_waitcnt lgkmcnt(4)\n"
"  v_mfma_f32_32x32x16_bf16 v[128:143], v[84:87], v[164:167], v[128:143]\n"
"  ds_read_b128 v[164:167], v234 offset:26624\n"
"  s_waitcnt lgkmcnt(4)\n"
"  v_mfma_f32_32x32x16_bf16 v[128:143], v[88:91], v[168:171], v[128:143]\n"
"  ds_read_b128 v[168:171], v234 offset:27648\n"
"  s_waitcnt lgkmcnt(4)\n"
"  v_mfma_f32_32x32x16_bf16 v[128:143], v[92:95], v[172:175], v[128:143]\n"
"  ds_read_b128 v[172:175], v234 offset:28672\n"
"  s_waitcnt lgkmcnt(4)\n"
"  v_mfma_f32_32x32x16_bf16 v[128:143], v[96:99], v[176:179], v[128:143]\n"
"  ds_read_b128 v[176:179], v234 offset:29696\n"
"  s_waitcnt lgkmcnt(4)\n"
"  v_mfma_f32_32x32x16_bf16 v[128:143], v[100:103], v[160:163], v[128:143]\n"
"  ds_read_b128 v[160:163], v234 offset:30720\n"
"  s_waitcnt lgkmcnt(4)\n"
"  v_mfma_f32_32x32x16_bf16 v[128:143], v[104:107], v[164:167], v[128:143]\n"
"  ds_read_b128 v[164:167], v234 offset:31744\n"
"  s_waitcnt lgkmcnt(4)\n"
"  v_mfma_f32_32x32x16_bf16 v[128:143], v[108:111], v[168:171], v[128:143]\n"
"  s_waitcnt lgkmcnt(3)\n"
"  v_mfma_f32_32x32x16_bf16 v[128:143], v[112:115], v[172:175], v[128:143]\n"
"  s_waitcnt lgkmcnt(2)\n"
"  v_mfma_f32_32x32x16_bf16 v[128:143], v[116:119], v[176:179], v[128:143]\n"
"  s_waitcnt lgkmcnt(1)\n"
"  v_mfma_f32_32x32x16_bf16 v[128:143], v[120:123], v[160:163], v[128:143]\n"
"  s_waitcnt lgkmcnt(0)\n"
"  v_mfma_f32_32x32x16_bf16 v[128:143], v[124:127], v[164:167], v[128:143]\n"
"  s_nop 11\n"
"  v_pk_add_f32 v[0:1], v[128:129], v[130:131]\n"
"  v_pk_mul_f32 v[2:3], v[128:129], v[128:129]\n"
"  v_pk_mul_f32 v[4:5], v[128:129], v[144:145]\n"
"  v_pk_fma_f32 v[2:3], v[130:131], v[130:131], v[2:3]\n"
"  v_pk_fma_f32 v[4:5], v[130:131], v[146:147], v[4:5]\n"
"  v_pk_add_f32 v[0:1], v[0:1], v[132:133]\n"
"  v_pk_fma_f32 v[2:3], v[132:133], v[132:133], v[2:3]\n"
"  v_pk_fma_f32 v[4:5], v[132:133], v[148:149], v[4:5]\n"
"  v_pk_add_f32 v[0:1], v[0:1], v[134:135]\n"
"  v_pk_fma_f32 v[2:3], v[134:135], v[134:135], v[2:3]\n"
"  v_pk_fma_f32 v[4:5], v[134:135], v[150:151], v[4:5]\n"
"  v_pk_add_f32 v[0:1], v[0:1], v[136:137]\n"
"  v_pk_fma_f32 v[2:3], v[136:137], v[136:137], v[2:3]\n"
"  v_pk_fma_f32 v[4:5], v[136:137], v[152:153], v[4:5]\n"
"  v_pk_add_f32 v[0:1], v[0:1], v[138:139]\n"
"  v_pk_fma_f32 v[2:3], v[138:139], v[138:139], v[2:3]\n"
"  v_pk_fma_f32 v[4:5], v[138:139], v[154:155], v[4:5]\n"
"  v_pk_add_f32 v[0:1], v[0:1], v[140:141]\n"
"  v_pk_fma_f32 v[2:3], v[140:141], v[140:141], v[2:3]\n"
"  v_pk_fma_f32 v[4:5], v[140:141], v[156:157], v[4:5]\n"
"  v_pk_add_f32 v[0:1], v[0:1], v[142:143]\n"
"  v_pk_fma_f32 v[2:3], v[142:143], v[142:143], v[2:3]\n"
"  v_pk_fma_f32 v[4:5], v[142:143], v[158:159], v[4:5]\n"
"  v_add_f32 v0, v0, v1\n"
"  v_add_f32 v1, v2, v3\n"
"  v_add_f32 v2, v4, v5\n"
"  ds_write_b96 v236, v[0:2]\n"
"  s_waitcnt lgkmcnt(0)\n"
"  s_barrier\n"
"  ds_write_b128 v237, v[128:131]\n"
"  ds_write_b128 v237, v[132:135] offset:32\n"
"  ds_write_b128 v237, v[136:139] offset:64\n"
"  ds_write_b128 v237, v[140:143] offset:96\n"
"  s_waitcnt lgkmcnt(0)\n"
"  s_barrier\n"
"  s_lshr_b32 s61, s47, 3\n"
"  s_mul_i32 s84, s46, 12288\n"
"  s_mul_i32 s60, s61, 384\n"
"  s_add_u32 s84, s84, s60\n"
"  s_add_u32 s84, s84, 196608\n"
"  s_add_u32 s85, s84, 6144\n"
"  v_lshrrev_b32 v178, 4, v255\n"
"  v_and_b32 v177, 3, v255\n"
"  v_lshl_or_b32 v178, v178, 2, v177\n"
"  v_bfe_u32 v177, v255, 2, 2\n"
"  v_mul_u32_u24 v177, 1536, v177\n"
"  v_lshl_add_u32 v176, v178, 4, v177\n"
"  v_lshl_add_u32 v177, v178, 3, v177\n"
"  s_lshl_b32 s60, s44, 11\n"
"  s_add_u32 s60, s60, 32768\n"
"  v_add_u32 v249, s60, v254\n"
"  s_waitcnt vmcnt(3)\n"
"  ds_write_b128 v249, v[212:215]\n"
"  s_waitcnt vmcnt(2)\n"
"  ds_write_b128 v249, v[216:219] offset:1024\n"
"  v_lshl_add_u32 v249, v255, 1, s60\n"
"  v_mov_b64 v[96:97], 0\n"
"  v_mov_b64 v[98:99], 0\n"
"  v_mov_b64 v[100:101], 0\n"
"  v_mov_b64 v[102:103], 0\n"
"  v_mov_b64 v[104:105], 0\n"
"  v_mov_b64 v[106:107], 0\n"
"  v_mov_b64 v[108:109], 0\n"
"  v_mov_b64 v[110:111], 0\n"
"  ds_read_u16_d16_hi v96, v249 offset:0\n"
"  ds_read_u16_d16_hi v97, v249 offset:128\n"
"  ds_read_u16_d16_hi v98, v249 offset:256\n"
"  ds_read_u16_d16_hi v99, v249 offset:384\n"
"  ds_read_u16_d16_hi v100, v249 offset:512\n"
"  ds_read_u16_d16_hi v101, v249 offset:640\n"
"  ds_read_u16_d16_hi v102, v249 offset:768\n"
"  ds_read_u16_d16_hi v103, v249 offset:896\n"
"  ds_read_u16_d16_hi v104, v249 offset:1024\n"
"  ds_read_u16_d16_hi v105, v249 offset:1152\n"
"  ds_read_u16_d16_hi v106, v249 offset:1280\n"
"  ds_read_u16_d16_hi v107, v249 offset:1408\n"
"  ds_read_u16_d16_hi v108, v249 offset:1536\n"
"  ds_read_u16_d16_hi v109, v249 offset:1664\n"
"  ds_read_u16_d16_hi v110, v249 offset:1792\n"
"  ds_read_u16_d16_hi v111, v249 offset:1920\n"
"  s_sleep 2\n"
"  buffer_load_dwordx4 v[212:215], v254, s[20:23], s80 offen sc1\n"
"  buffer_load_dwordx4 v[216:219], v254, s[20:23], s80 offen offset:1024 sc1\n"
".Lfb_M_stc:\n"
"  s_waitcnt vmcnt(1)\n"
"  v_add3_u32 v220, v212, v213, s56\n"
"  s_waitcnt vmcnt(0)\n"
"  v_add3_u32 v221, v216, v217, s56\n"
"  v_add_f32 v212, v212, v216\n"
"  v_add_u32 v220, v220, v214\n"
"  v_add_u32 v221, v221, v218\n"
"  v_add_f32 v213, v213, v217\n"
"  v_mov_b32 v216, v212\n"
"  v_cmp_ne_u32 vcc, v220, v215\n"
"  v_cmp_ne_u32 s[90:91], v221, v219\n"
"  v_add_f32 v214, v214, v218\n"
"  v_mov_b32 v217, v213\n"
"  s_or_b64 vcc, vcc, s[90:91]\n"
"  v_permlane32_swap_b32 v212, v216\n"
"  v_mov_b32 v218, v214\n"
"  s_cbranch_vccnz .Lfb_M_str\n"
".Lfb_M_stk:\n"
"  v_permlane32_swap_b32 v213, v217\n"
"  v_add_f32 v224, v212, v216\n"
"  v_permlane32_swap_b32 v214, v218\n"
"  v_mul_f32 v224, 0x3b000000, v224\n"
"  v_add_f32 v213, v213, v217\n"
"  v_mul_f32 v220, v224, v224\n"
"  v_add_f32 v214, v214, v218\n"
"  v_fma_f32 v220, v213, s81, -v220\n"
"  v_fma_f32 v214, -v224, v208, v214\n"
"  v_max_f32 v220, 0, v220\n"
"  v_add_f32 v220, 0x3727c5ac, v220\n"
"  v_rsq_f32 v225, v220\n"
"  s_nop 0\n"
"  v_mul_f32 v226, v214, v225\n"
"  v_ashrrev_i32 v220, 31, v226\n"
"  v_or_b32 v220, 0x80000000, v220\n"
"  v_xor_b32 v226, v226, v220\n"
"  v_and_or_b32 v226, v226, s82, v227\n"
"  s_nop 1\n"
"  v_max_u32_dpp v226, v226, v226 quad_perm:[1,0,3,2] row_mask:0xf bank_mask:0xf\n"
"  s_nop 1\n"
"  v_max_u32_dpp v226, v226, v226 quad_perm:[2,3,0,1] row_mask:0xf bank_mask:0xf\n"
"  s_nop 1\n"
"  v_max_u32_dpp v226, v226, v226 row_half_mirror row_mask:0xf bank_mask:0xf\n"
"  s_nop 1\n"
"  v_max_u32_dpp v226, v226, v226 row_mirror row_mask:0xf bank_mask:0xf\n"
"  v_mov_b32 v220, v226\n"
"  s_nop 1\n"
"  v_permlane16_swap_b32 v226, v220\n"
"  v_max3_u32 v226, v226, v220, 1\n"
"  ds_write_b32 v228, v226\n"
"  s_cmp_lg_u32 s44, 0\n"
"  s_cbranch_scc1 .Lfb_M_nopub\n"
"  v_readfirstlane_b32 s70, v226\n"
"  v_mul_f32 v224, -v224, v225\n"
"  s_andn2_b32 s60, 31, s70\n"
"  s_mul_i32 s61, s60, 528\n"
"  v_add_u32 v8, s61, v239\n"
"  ds_read_b128 v[0:3], v8\n"
"  ds_read_b128 v[4:7], v8 offset:16\n"
".Lfb_M_keys:\n"
"  ds_read_b128 v[16:19], v229\n"
"  ds_read_b128 v[20:23], v229 offset:16\n"
"  v_readlane_b32 s62, v224, s60\n"
"  v_readlane_b32 s63, v225, s60\n"
"  ds_write_b64 v238, v[224:225]\n"
"  v_mov_b32 v8, s62\n"
"  v_mov_b32 v9, s63\n"
"  v_mov_b32 v14, s70\n"
"  s_waitcnt lgkmcnt(4)\n"
"  v_pk_fma_f32 v[24:25], v[0:1], v[8:9], v[8:9] op_sel:[0,1,0] op_sel_hi:[1,1,0]\n"
"  v_pk_fma_f32 v[26:27], v[2:3], v[8:9], v[8:9] op_sel:[0,1,0] op_sel_hi:[1,1,0]\n"
"  s_waitcnt lgkmcnt(3)\n"
"  v_pk_fma_f32 v[28:29], v[4:5], v[8:9], v[8:9] op_sel:[0,1,0] op_sel_hi:[1,1,0]\n"
"  v_pk_fma_f32 v[30:31], v[6:7], v[8:9], v[8:9] op_sel:[0,1,0] op_sel_hi:[1,1,0]\n"
"  v_cvt_pk_bf16_f32 v10, v24, v25\n"
"  v_cvt_pk_bf16_f32 v11, v26, v27\n"
"  v_cvt_pk_bf16_f32 v12, v28, v29\n"
"  v_cvt_pk_bf16_f32 v13, v30, v31\n"
"  s_waitcnt lgkmcnt(2)\n"
"  v_max3_u32 v32, v16, v17, v18\n"
"  v_min3_u32 v33, v16, v17, v18\n"
"  v_add3_u32 v15, v10, v11, s56\n"
"  s_waitcnt lgkmcnt(1)\n"
"  v_max3_u32 v32, v32, v19, v20\n"
"  v_min3_u32 v33, v33, v19, v20\n"
"  v_add3_u32 v15, v15, v12, v13\n"
"  v_max3_u32 v32, v32, v21, v22\n"
"  v_min3_u32 v33, v33, v21, v22\n"
"  v_add_u32 v15, v15, v14\n"
"  v_max_u32 v32, v32, v23\n"
"  v_min_u32 v33, v33, v23\n"
"  v_cmp_ne_u32 s[90:91], v32, v14\n"
"  v_cmp_eq_u32 vcc, 0, v33\n"
"  s_cbranch_vccnz .Lfb_M_keyr\n"
".Lfb_M_keyk:\n"
"  s_and_b64 vcc, s[90:91], s[90:91]\n"
"  s_cbranch_scc1 .Lfb_M_nopub\n"
"  buffer_store_dwordx4 v[10:13], v240, s[20:23], s83 offen sc1\n"
"  buffer_store_dwordx2 v[14:15], v241, s[20:23], s83 offen offset:256 sc1\n"
"  buffer_store_dwordx4 v[10:13], v242, s[20:23], s83 offen sc1\n"
"  buffer_store_dwordx2 v[14:15], v243, s[20:23], s83 offen offset:256 sc1\n"
"  v_pk_add_f32 v[24:25], v[24:25], v[0:1]\n"
"  v_pk_add_f32 v[26:27], v[26:27], v[2:3]\n"
"  v_pk_add_f32 v[28:29], v[28:29], v[4:5]\n"
"  v_pk_add_f32 v[30:31], v[30:31], v[6:7]\n"
"  v_cvt_pk_bf16_f32 v16, v24, v25\n"
"  v_cvt_pk_bf16_f32 v17, v26, v27\n"
"  v_cvt_pk_bf16_f32 v18, v28, v29\n"
"  v_cvt_pk_bf16_f32 v19, v30, v31\n"
"  v_mov_b32 v20, v14\n"
"  v_add3_u32 v21, v16, v17, s56\n"
"  v_add3_u32 v21, v21, v18, v19\n"
"  v_lshrrev_b32 v22, 1, v254\n"
"  v_add_u32 v21, v21, v14\n"
"  s_mov_b64 exec, 0xffff\n"
"  buffer_store_dwordx4 v[16:19], v254, s[20:23], s83 offen sc1\n"
"  buffer_store_dwordx2 v[20:21], v22, s[20:23], s83 offen offset:256 sc1\n"
"  s_mov_b64 exec, -1\n"
".Lfb_M_nopub:\n"
"  s_waitcnt lgkmcnt(0)\n"
"  s_barrier\n"
"  ds_read_b128 v[16:19], v229\n"
"  ds_read_b128 v[20:23], v229 offset:16\n"
"  ds_read_b128 v[40:43], v244\n"
"  ds_read_b128 v[44:47], v244 offset:16\n"
"  ds_read_b64 v[56:57], v245\n"
"  ds_read_b128 v[48:51], v244 offset:8448\n"
"  ds_read_b128 v[52:55], v244 offset:8464\n"
"  ds_read_b64 v[58:59], v245 offset:128\n"
"  s_waitcnt lgkmcnt(7)\n"
"  v_max3_u32 v32, v16, v17, v18\n"
"  s_waitcnt lgkmcnt(6)\n"
"  v_max3_u32 v32, v32, v19, v20\n"
"  v_max3_u32 v32, v32, v21, v22\n"
"  v_max_u32 v32, v32, v23\n"
"  s_nop 0\n"
"  v_readfirstlane_b32 s71, v32\n"
"  s_movk_i32 s79, 0x4000\n"
"  s_andn2_b32 s60, 2047, s71\n"
"  s_sub_u32 s61, s60, 16\n"
"  v_cmp_eq_u32 s[92:93], s60, v247\n"
"  v_cmp_eq_u32 s[94:95], s61, v247\n"
"  s_waitcnt lgkmcnt(3)\n"
"  v_add_f32 v57, 1.0, v57\n"
"  v_pk_fma_f32 v[40:41], v[40:41], v[56:57], v[56:57] op_sel:[0,1,0] op_sel_hi:[1,1,0]\n"
"  v_pk_fma_f32 v[42:43], v[42:43], v[56:57], v[56:57] op_sel:[0,1,0] op_sel_hi:[1,1,0]\n"
"  v_pk_fma_f32 v[44:45], v[44:45], v[56:57], v[56:57] op_sel:[0,1,0] op_sel_hi:[1,1,0]\n"
"  v_pk_fma_f32 v[46:47], v[46:47], v[56:57], v[56:57] op_sel:[0,1,0] op_sel_hi:[1,1,0]\n"
"  v_cvt_pk_bf16_f32 v60, v40, v41\n"
"  v_cvt_pk_bf16_f32 v61, v42, v43\n"
"  v_cvt_pk_bf16_f32 v62, v44, v45\n"
"  v_cvt_pk_bf16_f32 v63, v46, v47\n"
"  s_waitcnt lgkmcnt(0)\n"
"  v_add_f32 v59, 1.0, v59\n"
"  v_pk_fma_f32 v[48:49], v[48:49], v[58:59], v[58:59] op_sel:[0,1,0] op_sel_hi:[1,1,0]\n"
"  v_pk_fma_f32 v[50:51], v[50:51], v[58:59], v[58:59] op_sel:[0,1,0] op_sel_hi:[1,1,0]\n"
"  v_pk_fma_f32 v[52:53], v[52:53], v[58:59], v[58:59] op_sel:[0,1,0] op_sel_hi:[1,1,0]\n"
"  v_pk_fma_f32 v[54:55], v[54:55], v[58:59], v[58:59] op_sel:[0,1,0] op_sel_hi:[1,1,0]\n"
"  v_cvt_pk_bf16_f32 v64, v48, v49\n"
"  v_cvt_pk_bf16_f32 v65, v50, v51\n"
"  v_cvt_pk_bf16_f32 v66, v52, v53\n"
"  v_cvt_pk_bf16_f32 v67, v54, v55\n"
"  s_cmp_eq_u32 s46, 0\n"
"  s_cbranch_scc1 .Lfb_M_g0\n"
"  s_not_b64 exec, s[92:93]\n"
"  buffer_store_dwordx4 v[60:63], v246, s[36:39], 0 offen\n"
"  s_not_b64 exec, s[94:95]\n"
"  buffer_store_dwordx4 v[64:67], v246, s[36:39], s79 offen\n"
"  s_mov_b64 exec, -1\n"
"  s_cmp_eq_u32 s3, 0\n"
"  s_cbranch_scc1 .Lfb_M_np\n"
"  s_sub_u32 s60, s46, 1\n"
"  s_lshl_b32 s60, s60, 5\n"
"  s_add_u32 s60, s60, s47\n"
"  s_lshr_b32 s60, s60, 1\n"
"  s_mul_i32 s75, s44, 224\n"
"  s_add_u32 s75, s75, s60\n"
"  s_lshl_b32 s75, s75, 7\n"
"  s_add_u32 s75, s75, 655360\n"
"  v_lshrrev_b32 v112, 5, v255\n"
"  v_mul_u32_u24 v112, 14336, v112\n"
"  v_and_b32 v113, 0x7c, v253\n"
"  v_add_u32 v113, v113, v112\n"
"  v_and_b32 v112, 15, v255\n"
"  v_lshlrev_b32 v112, 5, v112\n"
"  v_add_u32 v112, 157696, v112\n"
"  v_and_b32 v114, 1, v255\n"
"  v_mul_lo_u32 v114, v114, s56\n"
"  s_mov_b32 s61, 0xbfb8aa3b\n"
"  s_barrier\n"
"  ds_read_b128 v[116:119], v112\n"
"  ds_read_b128 v[120:123], v112 offset:16\n"
"  s_waitcnt lgkmcnt(1)\n"
"  v_pk_add_f32 v[116:117], v[116:117], v[118:119]\n"
"  s_waitcnt lgkmcnt(0)\n"
"  v_pk_add_f32 v[120:121], v[120:121], v[122:123]\n"
"  v_pk_add_f32 v[116:117], v[116:117], v[120:121]\n"
"  v_mul_f32 v118, s61, v116\n"
"  v_exp_f32 v118, v118\n"
"  v_mul_f32 v116, v116, v117\n"
"  v_add_f32 v118, 1.0, v118\n"
"  v_rcp_f32 v118, v118\n"
"  s_nop 0\n"
"  v_mul_f32 v116, v116, v118\n"
"  s_nop 1\n"
"  v_mul_f32_dpp v120, v116, v96 row_newbcast:0 row_mask:0xf bank_mask:0xf\n"
"  v_mul_f32_dpp v121, v116, v97 row_newbcast:1 row_mask:0xf bank_mask:0xf\n"
"  v_mul_f32_dpp v122, v116, v98 row_newbcast:2 row_mask:0xf bank_mask:0xf\n"
"  v_mul_f32_dpp v123, v116, v99 row_newbcast:3 row_mask:0xf bank_mask:0xf\n"
"  v_fmac_f32_dpp v120, v116, v100 row_newbcast:4 row_mask:0xf bank_mask:0xf\n"
"  v_fmac_f32_dpp v121, v116, v101 row_newbcast:5 row_mask:0xf bank_mask:0xf\n"
"  v_fmac_f32_dpp v122, v116, v102 row_newbcast:6 row_mask:0xf bank_mask:0xf\n"
"  v_fmac_f32_dpp v123, v116, v103 row_newbcast:7 row_mask:0xf bank_mask:0xf\n"
"  v_fmac_f32_dpp v120, v116, v104 row_newbcast:8 row_mask:0xf bank_mask:0xf\n"
"  v_fmac_f32_dpp v121, v116, v105 row_newbcast:9 row_mask:0xf bank_mask:0xf\n"
"  v_fmac_f32_dpp v122, v116, v106 row_newbcast:10 row_mask:0xf bank_mask:0xf\n"
"  v_fmac_f32_dpp v123, v116, v107 row_newbcast:11 row_mask:0xf bank_mask:0xf\n"
"  v_fmac_f32_dpp v120, v116, v108 row_newbcast:12 row_mask:0xf bank_mask:0xf\n"
"  v_fmac_f32_dpp v121, v116, v109 row_newbcast:13 row_mask:0xf bank_mask:0xf\n"
"  v_fmac_f32_dpp v122, v116, v110 row_newbcast:14 row_mask:0xf bank_mask:0xf\n"
"  v_fmac_f32_dpp v123, v116, v111 row_newbcast:15 row_mask:0xf bank_mask:0xf\n"
"  v_pk_add_f32 v[120:121], v[120:121], v[122:123]\n"
"  v_add_f32 v116, v120, v121\n"
"  s_nop 1\n"
"  v_mov_b32_dpp v117, v116 quad_perm:[0,0,2,2] row_mask:0xf bank_mask:0xf\n"
"  v_mov_b32_dpp v118, v116 quad_perm:[1,1,3,3] row_mask:0xf bank_mask:0xf\n"
"  v_cvt_pk_bf16_f32 v116, v117, v118\n"
"  v_add_u32 v116, v116, v114\n"
"  buffer_store_dword v116, v113, s[20:23], s75 offen sc1\n"
"  s_branch .Lfb_M_held\n"
".Lfb_M_np:\n"
"  s_lshr_b32 s60, s60, 5\n"
"  s_cmp_lg_u32 s60, s52\n"
"  s_cbranch_scc1 .Lfb_M_end\n"
"  s_branch .Lfb_M_ca\n"
".Lfb_M_g0:\n"
"  s_not_b64 exec, s[92:93]\n"
"  buffer_store_dwordx4 v[60:63], v246, s[36:39], 0 offen sc1\n"
"  s_not_b64 exec, s[94:95]\n"
"  buffer_store_dwordx4 v[64:67], v246, s[36:39], s79 offen sc1\n"
"  s_mov_b64 exec, -1\n"
"  s_lshr_b32 s60, s47, 1\n"
"  s_mul_i32 s60, s60, 7\n"
"  s_add_u32 s60, s60, s44\n"
"  s_lshl_b32 s60, s60, 5\n"
"  s_and_b32 s61, s47, 1\n"
"  s_add_u32 s60, s60, s61\n"
"  s_lshl_b32 s60, s60, 6\n"
"  s_add_u32 s75, s60, 653312\n"
"  v_lshrrev_b32 v112, 2, v255\n"
"  v_and_b32 v113, 0x30, v254\n"
"  v_lshl_or_b32 v112, v112, 7, v113\n"
"  v_bfe_u32 v114, v255, 4, 1\n"
"  v_lshrrev_b32 v113, 5, v255\n"
"  v_lshl_or_b32 v113, v114, 1, v113\n"
"  v_and_b32 v114, 3, v255\n"
"  v_lshl_or_b32 v113, v114, 2, v113\n"
"  s_lshl2_add_u32 s60, s44, 157696\n"
"  v_lshl_add_u32 v113, v113, 5, s60\n"
".Lfb_M_ca:\n"
"  s_cmp_lg_u32 s44, 0\n"
"  s_cbranch_scc1 .Lfb_M_d1\n"
"  buffer_load_dwordx2 v[168:169], v177, s[20:23], s84 offen offset:256 sc1\n"
"  buffer_load_dwordx2 v[170:171], v177, s[20:23], s85 offen offset:256 sc1\n"
"  buffer_load_dwordx4 v[160:163], v176, s[20:23], s84 offen sc1\n"
"  buffer_load_dwordx4 v[164:167], v176, s[20:23], s85 offen sc1\n"
".Lfb_M_cac:\n"
"  s_waitcnt vmcnt(2)\n"
"  v_max_u32 v174, v168, v170\n"
"  v_add_u32 v172, s56, v168\n"
"  v_add_u32 v173, s56, v170\n"
"  v_max_u32_dpp v174, v174, v174 row_ror:4 row_mask:0xf bank_mask:0xf\n"
"  s_waitcnt vmcnt(1)\n"
"  v_add3_u32 v172, v172, v160, v161\n"
"  v_add3_u32 v172, v172, v162, v163\n"
"  v_max_u32_dpp v174, v174, v174 row_ror:8 row_mask:0xf bank_mask:0xf\n"
"  v_cmp_ne_u32 vcc, v172, v169\n"
"  v_readfirstlane_b32 s86, v174\n"
"  s_waitcnt vmcnt(0)\n"
"  v_add3_u32 v173, v173, v164, v165\n"
"  v_add3_u32 v173, v173, v166, v167\n"
"  v_cmp_ne_u32 s[90:91], v173, v171\n"
"  s_andn2_b32 s87, 2047, s86\n"
"  s_or_b64 vcc, vcc, s[90:91]\n"
"  s_lshr_b32 s87, s87, 8\n"
"  s_cbranch_vccnz .Lfb_M_car\n"
".Lfb_M_cak:\n"
"  s_cmp_lt_u32 s87, 4\n"
"  s_cbranch_scc1 .Lfb_M_cal\n"
"  v_mov_b64 v[160:161], v[164:165]\n"
"  v_mov_b64 v[162:163], v[166:167]\n"
".Lfb_M_cal:\n"
"  v_mov_b32 v0, s86\n"
"  ds_write_b32 v229, v0 offset:32\n"
".Lfb_M_d1:\n"
"  s_waitcnt lgkmcnt(0)\n"
"  s_barrier\n"
"  s_cmp_lg_u32 s46, 0\n"
"  s_cbranch_scc1 .Lfb_M_held\n"
"  s_cmp_eq_u32 s44, 0\n"
"  s_cbranch_scc1 .Lfb_M_fin\n"
"  s_sleep 14\n"
"  buffer_load_dwordx4 v[116:119], v112, s[20:23], s75 offen sc1\n"
".Lfb_M_acc:\n"
"  s_waitcnt vmcnt(0)\n"
"  v_sub_u32 v117, v117, v116\n"
"  v_sub_u32 v119, v119, v118\n"
"  v_lshlrev_b32 v120, 16, v116\n"
"  v_and_b32 v121, s59, v116\n"
"  v_cmp_ne_u32 vcc, s56, v117\n"
"  v_cmp_ne_u32 s[90:91], s56, v119\n"
"  v_lshlrev_b32 v122, 16, v118\n"
"  v_and_b32 v123, s59, v118\n"
"  v_permlane32_swap_b32 v120, v121\n"
"  s_or_b64 vcc, vcc, s[90:91]\n"
"  v_permlane32_swap_b32 v122, v123\n"
"  s_cbranch_vccnz .Lfb_M_acr\n"
".Lfb_M_ack:\n"
"  v_add_f32 v120, v120, v121\n"
"  v_add_f32 v122, v122, v123\n"
"  s_nop 1\n"
"  v_permlane16_swap_b32 v120, v122\n"
"  v_add_f32 v120, v120, v122\n"
"  s_nop 1\n"
"  v_add_f32_dpp v120, v120, v120 row_ror:8 row_mask:0xf bank_mask:0xf\n"
"  s_nop 1\n"
"  v_add_f32_dpp v120, v120, v120 row_ror:4 row_mask:0xf bank_mask:0xf\n"
"  ds_write_b32 v113, v120\n"
"  s_waitcnt lgkmcnt(0)\n"
"  s_barrier\n"
"  s_branch .Lfb_M_held\n"
".Lfb_M_fin:\n"
"  s_and_b32 s60, s87, 3\n"
"  s_and_b32 s61, s47, 7\n"
"  v_bfe_u32 v1, v255, 2, 2\n"
"  v_lshrrev_b32 v2, 1, v178\n"
"  v_cmp_eq_u32 vcc, s60, v1\n"
"  v_cmp_eq_u32 s[90:91], s61, v2\n"
"  v_and_b32 v1, 16, v254\n"
"  v_add_u32 v1, 156928, v1\n"
"  s_and_b64 exec, vcc, s[90:91]\n"
"  ds_write_b128 v1, v[160:163]\n"
"  s_mov_b64 exec, -1\n"
"  v_and_b32 v2, 15, v255\n"
"  v_lshlrev_b32 v31, 1, v2\n"
"  v_add_u32 v3, 156928, v31\n"
"  ds_read_u16 v29, v3\n"
"  v_and_b32 v0, s82, v0\n"
"  v_ashrrev_i32 v1, 31, v0\n"
"  v_not_b32 v1, v1\n"
"  v_or_b32 v1, 0x80000000, v1\n"
"  v_xor_b32 v0, v0, v1\n"
"  v_mul_f32 v0, 0xbfb8aa3b, v0\n"
"  v_exp_f32 v0, v0\n"
"  v_lshlrev_b32 v30, 5, v2\n"
"  v_add_f32 v0, 1.0, v0\n"
"  v_rcp_f32 v28, v0\n"
"  v_add_u32 v30, 157696, v30\n"
"  s_waitcnt lgkmcnt(0)\n"
"  v_lshlrev_b32 v29, 16, v29\n"
"  s_andn2_b32 s60, 2047, s86\n"
"  s_lshl_b32 s60, s60, 5\n"
"  s_add_u32 s60, s60, s47\n"
"  s_lshl_b32 s62, s60, 5\n"
"  s_cmp_lg_u32 s2, 0\n"
"  s_cbranch_scc1 .Lfb_M_d2\n"
"  s_add_u32 s60, s57, 1\n"
"  v_mov_b32 v0, s60\n"
"  s_mov_b64 exec, 1\n"
"  buffer_store_dword v0, off, s[20:23], 0 sc1\n"
".Lfb_M_d2:\n"
"  s_mov_b64 exec, 0xffff\n"
"  s_barrier\n"
"  ds_read_b128 v[0:3], v30\n"
"  ds_read_b128 v[4:7], v30 offset:16\n"
"  s_waitcnt lgkmcnt(1)\n"
"  v_add_f32 v1, v1, v2\n"
"  s_waitcnt lgkmcnt(0)\n"
"  v_pk_add_f32 v[4:5], v[4:5], v[6:7]\n"
"  v_add_f32 v1, v1, v3\n"
"  v_add_f32 v4, v4, v5\n"
"  v_add_f32 v1, v1, v4\n"
"  v_fmac_f32 v29, v28, v1\n"
"  v_cvt_pk_bf16_f32 v0, v29, v29\n"
"  buffer_store_short v0, v31, s[36:39], s62 offen sc1\n"
"  s_mov_b64 exec, -1\n"
".Lfb_M_held:\n"
"  s_or_b64 vcc, s[92:93], s[94:95]\n"
"  s_cbranch_scc0 .Lfb_M_end\n"
"  ds_read_b32 v4, v229 offset:32\n"
"  s_waitcnt lgkmcnt(0)\n"
"  v_cmp_ne_u32 vcc, s71, v4\n"
"  s_nop 0\n"
"  s_and_b64 exec, vcc, s[92:93]\n"
"  s_cbranch_execz .Lfb_M_h1\n"
"  buffer_store_dwordx4 v[60:63], v246, s[36:39], 0 offen\n"
".Lfb_M_h1:\n"
"  s_and_b64 exec, vcc, s[94:95]\n"
"  s_cbranch_execz .Lfb_M_h2\n"
"  buffer_store_dwordx4 v[64:67], v246, s[36:39], s79 offen\n"
".Lfb_M_h2:\n"
"  s_mov_b64 exec, -1\n"
".Lfb_M_end:\n"
"  s_endpgm\n"
".Lfb_slow:\n"
"  s_lshl_b32 s64, s48, 1\n"
"  s_sub_i32 s64, 8, s64\n"
"  s_max_i32 s64, s64, s54\n"
"  s_add_u32 s65, s54, 8\n"
"  s_mov_b32 s66, 0\n"
"  s_waitcnt lgkmcnt(3)\n"
"  v_mov_b32 v100, 0\n"
"  v_mov_b32 v103, 4286578688\n"
"  v_mov_b32 v72, 4286578688\n"
".Lfb_slow_pass:\n"
"  s_mov_b32 s63, s64\n"
".Lfb_slow_tile:\n"
"  s_lshl_b32 s67, s63, 13\n"
"  v_add_u32 v102, s67, v200\n"
"  ds_read_b128 v[80:83], v102\n"
"  v_mad_u32_u24 v102, v202, s63, v203\n"
"  ds_read_b128 v[84:87], v102\n"
"  ds_read_b128 v[88:91], v102 offset:1024\n"
"  s_sub_u32 s67, s63, s54\n"
"  s_lshl_b32 s67, s67, 5\n"
"  s_sub_u32 s67, s67, 1\n"
"  v_sub_u32 v101, s67, v205\n"
"  s_movk_i32 s67, 0x100\n"
"  v_add_u32 v102, 0, v101\n"
"  v_cmp_gt_u32 vcc, s67, v102\n"
"  s_nop 1\n"
"  v_cndmask_b32 v48, v103, v100, vcc\n"
"  v_add_u32 v102, 1, v101\n"
"  v_cmp_gt_u32 vcc, s67, v102\n"
"  s_nop 1\n"
"  v_cndmask_b32 v49, v103, v100, vcc\n"
"  v_add_u32 v102, 2, v101\n"
"  v_cmp_gt_u32 vcc, s67, v102\n"
"  s_nop 1\n"
"  v_cndmask_b32 v50, v103, v100, vcc\n"
"  v_add_u32 v102, 3, v101\n"
"  v_cmp_gt_u32 vcc, s67, v102\n"
"  s_nop 1\n"
"  v_cndmask_b32 v51, v103, v100, vcc\n"
"  v_add_u32 v102, 8, v101\n"
"  v_cmp_gt_u32 vcc, s67, v102\n"
"  s_nop 1\n"
"  v_cndmask_b32 v52, v103, v100, vcc\n"
"  v_add_u32 v102, 9, v101\n"
"  v_cmp_gt_u32 vcc, s67, v102\n"
"  s_nop 1\n"
"  v_cndmask_b32 v53, v103, v100, vcc\n"
"  v_add_u32 v102, 10, v101\n"
"  v_cmp_gt_u32 vcc, s67, v102\n"
"  s_nop 1\n"
"  v_cndmask_b32 v54, v103, v100, vcc\n"
"  v_add_u32 v102, 11, v101\n"
"  v_cmp_gt_u32 vcc, s67, v102\n"
"  s_nop 1\n"
"  v_cndmask_b32 v55, v103, v100, vcc\n"
"  v_add_u32 v102, 16, v101\n"
"  v_cmp_gt_u32 vcc, s67, v102\n"
"  s_nop 1\n"
"  v_cndmask_b32 v56, v103, v100, vcc\n"
"  v_add_u32 v102, 17, v101\n"
"  v_cmp_gt_u32 vcc, s67, v102\n"
"  s_nop 1\n"
"  v_cndmask_b32 v57, v103, v100, vcc\n"
"  v_add_u32 v102, 18, v101\n"
"  v_cmp_gt_u32 vcc, s67, v102\n"
"  s_nop 1\n"
"  v_cndmask_b32 v58, v103, v100, vcc\n"
"  v_add_u32 v102, 19, v101\n"
"  v_cmp_gt_u32 vcc, s67, v102\n"
"  s_nop 1\n"
"  v_cndmask_b32 v59, v103, v100, vcc\n"
"  v_add_u32 v102, 24, v101\n"
"  v_cmp_gt_u32 vcc, s67, v102\n"
"  s_nop 1\n"
"  v_cndmask_b32 v60, v103, v100, vcc\n"
"  v_add_u32 v102, 25, v101\n"
"  v_cmp_gt_u32 vcc, s67, v102\n"
"  s_nop 1\n"
"  v_cndmask_b32 v61, v103, v100, vcc\n"
"  v_add_u32 v102, 26, v101\n"
"  v_cmp_gt_u32 vcc, s67, v102\n"
"  s_nop 1\n"
"  v_cndmask_b32 v62, v103, v100, vcc\n"
"  v_add_u32 v102, 27, v101\n"
"  v_cmp_gt_u32 vcc, s67, v102\n"
"  s_nop 1\n"
"  v_cndmask_b32 v63, v103, v100, vcc\n"
"  s_waitcnt lgkmcnt(2)\n"
"  s_nop 0\n"
"  v_mfma_f32_32x32x16_bf16 v[32:47], v[80:83], v[196:199], v[48:63]\n"
"  s_cmp_eq_u32 s66, 0\n"
"  s_cbranch_scc0 .Lfb_slow_pv\n"
"  s_nop 9\n"
"  v_max3_f32 v72, v72, v32, v33\n"
"  v_max3_f32 v72, v72, v34, v35\n"
"  v_max3_f32 v72, v72, v36, v37\n"
"  v_max3_f32 v72, v72, v38, v39\n"
"  v_max3_f32 v72, v72, v40, v41\n"
"  v_max3_f32 v72, v72, v42, v43\n"
"  v_max3_f32 v72, v72, v44, v45\n"
"  v_max3_f32 v72, v72, v46, v47\n"
"  s_branch .Lfb_slow_next\n"
".Lfb_slow_pv:\n"
"  s_nop 9\n"
"  v_exp_f32 v32, v32\n"
"  v_exp_f32 v33, v33\n"
"  v_exp_f32 v34, v34\n"
"  v_exp_f32 v35, v35\n"
"  v_cvt_pk_bf16_f32 v32, v32, v33\n"
"  v_exp_f32 v36, v36\n"
"  v_exp_f32 v37, v37\n"
"  v_cvt_pk_bf16_f32 v33, v34, v35\n"
"  v_exp_f32 v38, v38\n"
"  v_exp_f32 v39, v39\n"
"  v_cvt_pk_bf16_f32 v34, v36, v37\n"
"  v_exp_f32 v40, v40\n"
"  v_cvt_pk_bf16_f32 v35, v38, v39\n"
"  v_exp_f32 v41, v41\n"
"  v_exp_f32 v42, v42\n"
"  s_waitcnt lgkmcnt(1)\n"
"  v_mfma_f32_32x32x16_bf16 v[64:79], v[84:87], v[32:35], v[64:79]\n"
"  v_exp_f32 v43, v43\n"
"  v_cvt_pk_bf16_f32 v40, v40, v41\n"
"  v_exp_f32 v44, v44\n"
"  v_exp_f32 v45, v45\n"
"  v_cvt_pk_bf16_f32 v41, v42, v43\n"
"  v_exp_f32 v46, v46\n"
"  v_exp_f32 v47, v47\n"
"  v_cvt_pk_bf16_f32 v42, v44, v45\n"
"  v_cvt_pk_bf16_f32 v43, v46, v47\n"
"  s_waitcnt lgkmcnt(0)\n"
"  s_nop 0\n"
"  v_mfma_f32_32x32x16_bf16 v[64:79], v[88:91], v[40:43], v[64:79]\n"
".Lfb_slow_next:\n"
"  s_add_u32 s63, s63, 1\n"
"  s_cmp_le_u32 s63, s65\n"
"  s_cbranch_scc1 .Lfb_slow_tile\n"
"  s_cmp_eq_u32 s66, 0\n"
"  s_cbranch_scc0 .Lfb_slow_done\n"
"  s_nop 6\n"
"  v_mov_b32 v102, v72\n"
"  s_nop 1\n"
"  v_permlane32_swap_b32 v72, v102\n"
"  v_max_f32 v72, v72, v102\n"
"  v_xor_b32 v100, 0x80000000, v72\n"
"  s_mov_b32 s66, 1\n"
"  v_mov_b32 v64, 0\n"
"  v_mov_b32 v65, 0\n"
"  v_mov_b32 v66, 0\n"
"  v_mov_b32 v67, 0\n"
"  v_mov_b32 v68, 0\n"
"  v_mov_b32 v69, 0\n"
"  v_mov_b32 v70, 0\n"
"  v_mov_b32 v71, 0\n"
"  v_mov_b32 v72, 0\n"
"  v_mov_b32 v73, 0\n"
"  v_mov_b32 v74, 0\n"
"  v_mov_b32 v75, 0\n"
"  v_mov_b32 v76, 0\n"
"  v_mov_b32 v77, 0\n"
"  v_mov_b32 v78, 0\n"
"  v_mov_b32 v79, 0\n"
"  s_branch .Lfb_slow_pass\n"
".Lfb_slow_done:\n"
"  s_cmp_lt_u32 s44, 4\n"
"  s_cbranch_scc1 .Lfb_M_norm\n"
"  s_branch .Lfb_L_norm\n"
".Lfb_kvr0:\n"
"  s_add_u32 s58, s58, 1\n"
"  s_cmp_gt_u32 s58, 0x20000\n"
"  s_cbranch_scc1 .Lfb_L_kvk0\n"
"  s_add_u32 m0, s80, 49152\n"
"  buffer_load_dword v132, v239, s[20:23], s76 offen sc1\n"
"  buffer_load_dwordx4 v254, s[20:23], s76 offen sc1 lds\n"
"  buffer_load_dwordx4 v254, s[20:23], s76 offen offset:1024 sc1 lds\n"
"  buffer_load_dwordx4 v254, s[20:23], s76 offen offset:2048 sc1 lds\n"
"  buffer_load_dwordx4 v254, s[20:23], s76 offen offset:3072 sc1 lds\n"
"  s_waitcnt vmcnt(0)\n"
"  ds_read_b128 v[100:103], v245 offset:49152\n"
"  ds_read_b128 v[104:107], v245 offset:50176\n"
"  ds_read_b128 v[108:111], v245 offset:51200\n"
"  ds_read_b128 v[112:115], v245 offset:52224\n"
"  s_waitcnt vmcnt(0)\n"
"  s_branch .Lfb_kvc0\n"
".Lfb_kvr1:\n"
"  s_add_u32 s58, s58, 1\n"
"  s_cmp_gt_u32 s58, 0x20000\n"
"  s_cbranch_scc1 .Lfb_L_kvk1\n"
"  s_add_u32 m0, s80, 32768\n"
"  buffer_load_dword v133, v239, s[20:23], s77 offen sc1\n"
"  buffer_load_dwordx4 v254, s[20:23], s77 offen sc1 lds\n"
"  buffer_load_dwordx4 v254, s[20:23], s77 offen offset:1024 sc1 lds\n"
"  buffer_load_dwordx4 v254, s[20:23], s77 offen offset:2048 sc1 lds\n"
"  buffer_load_dwordx4 v254, s[20:23], s77 offen offset:3072 sc1 lds\n"
"  s_waitcnt vmcnt(0)\n"
"  ds_read_b128 v[116:119], v245 offset:32768\n"
"  ds_read_b128 v[120:123], v245 offset:33792\n"
"  ds_read_b128 v[124:127], v245 offset:34816\n"
"  ds_read_b128 v[128:131], v245 offset:35840\n"
"  s_waitcnt vmcnt(0)\n"
"  s_branch .Lfb_kvc1\n"
".Lfb_kvr2:\n"
"  s_add_u32 s58, s58, 1\n"
"  s_cmp_gt_u32 s58, 0x20000\n"
"  s_cbranch_scc1 .Lfb_L_kvk2\n"
"  s_add_u32 m0, s80, 16384\n"
"  buffer_load_dword v134, v239, s[20:23], s78 offen sc1\n"
"  buffer_load_dwordx4 v254, s[20:23], s78 offen sc1 lds\n"
"  buffer_load_dwordx4 v254, s[20:23], s78 offen offset:1024 sc1 lds\n"
"  buffer_load_dwordx4 v254, s[20:23], s78 offen offset:2048 sc1 lds\n"
"  buffer_load_dwordx4 v254, s[20:23], s78 offen offset:3072 sc1 lds\n"
"  s_waitcnt vmcnt(0)\n"
"  ds_read_b128 v[100:103], v245 offset:16384\n"
"  ds_read_b128 v[104:107], v245 offset:17408\n"
"  ds_read_b128 v[108:111], v245 offset:18432\n"
"  ds_read_b128 v[112:115], v245 offset:19456\n"
"  s_waitcnt vmcnt(0)\n"
"  s_branch .Lfb_kvc2\n"
".Lfb_kvr3:\n"
"  s_add_u32 s58, s58, 1\n"
"  s_cmp_gt_u32 s58, 0x20000\n"
"  s_cbranch_scc1 .Lfb_L_kvk3\n"
"  s_add_u32 m0, s80, 0\n"
"  buffer_load_dword v135, v239, s[20:23], s79 offen sc1\n"
"  buffer_load_dwordx4 v254, s[20:23], s79 offen sc1 lds\n"
"  buffer_load_dwordx4 v254, s[20:23], s79 offen offset:1024 sc1 lds\n"
"  buffer_load_dwordx4 v254, s[20:23], s79 offen offset:2048 sc1 lds\n"
"  buffer_load_dwordx4 v254, s[20:23], s79 offen offset:3072 sc1 lds\n"
"  s_waitcnt vmcnt(0)\n"
"  ds_read_b128 v[116:119], v245\n"
"  ds_read_b128 v[120:123], v245 offset:1024\n"
"  ds_read_b128 v[124:127], v245 offset:2048\n"
"  ds_read_b128 v[128:131], v245 offset:3072\n"
"  s_waitcnt vmcnt(0)\n"
"  s_branch .Lfb_kvc3\n"
".Lfb_or0:\n"
"  s_add_u32 s58, s58, 1\n"
"  s_cmp_gt_u32 s58, 0x20000\n"
"  s_cbranch_scc1 .Lfb_ok0\n"
"  s_add_u32 m0, s84, 0\n"
"  buffer_load_dword v32, v253, s[20:23], s68 offen offset:1024 sc1\n"
"  buffer_load_dword v33, v253, s[20:23], s68 offen offset:2304 sc1\n"
"  buffer_load_dwordx4 v254, s[20:23], s68 offen sc1 lds\n"
"  buffer_load_dwordx4 v245, s[20:23], s68 offen offset:1024 sc1 lds\n"
"  s_add_u32 m0, s84, 8192\n"
"  buffer_load_dword v34, v253, s[20:23], s69 offen offset:1024 sc1\n"
"  buffer_load_dword v35, v253, s[20:23], s69 offen offset:2304 sc1\n"
"  buffer_load_dwordx4 v254, s[20:23], s69 offen sc1 lds\n"
"  buffer_load_dwordx4 v245, s[20:23], s69 offen offset:1024 sc1 lds\n"
"  s_add_u32 m0, s84, 16384\n"
"  buffer_load_dword v36, v253, s[20:23], s70 offen offset:1024 sc1\n"
"  buffer_load_dword v37, v253, s[20:23], s70 offen offset:2304 sc1\n"
"  buffer_load_dwordx4 v254, s[20:23], s70 offen sc1 lds\n"
"  buffer_load_dwordx4 v245, s[20:23], s70 offen offset:1024 sc1 lds\n"
"  s_add_u32 m0, s84, 24576\n"
"  buffer_load_dword v38, v253, s[20:23], s71 offen offset:1024 sc1\n"
"  buffer_load_dword v39, v253, s[20:23], s71 offen offset:2304 sc1\n"
"  buffer_load_dwordx4 v254, s[20:23], s71 offen sc1 lds\n"
"  buffer_load_dwordx4 v245, s[20:23], s71 offen offset:1024 sc1 lds\n"
"  s_branch .Lfb_oc0\n"
".Lfb_or1:\n"
"  s_add_u32 s58, s58, 1\n"
"  s_cmp_gt_u32 s58, 0x20000\n"
"  s_cbranch_scc1 .Lfb_ok1\n"
"  s_add_u32 m0, s84, 8192\n"
"  buffer_load_dword v34, v253, s[20:23], s69 offen offset:1024 sc1\n"
"  buffer_load_dword v35, v253, s[20:23], s69 offen offset:2304 sc1\n"
"  buffer_load_dwordx4 v254, s[20:23], s69 offen sc1 lds\n"
"  buffer_load_dwordx4 v245, s[20:23], s69 offen offset:1024 sc1 lds\n"
"  s_add_u32 m0, s84, 16384\n"
"  buffer_load_dword v36, v253, s[20:23], s70 offen offset:1024 sc1\n"
"  buffer_load_dword v37, v253, s[20:23], s70 offen offset:2304 sc1\n"
"  buffer_load_dwordx4 v254, s[20:23], s70 offen sc1 lds\n"
"  buffer_load_dwordx4 v245, s[20:23], s70 offen offset:1024 sc1 lds\n"
"  s_add_u32 m0, s84, 24576\n"
"  buffer_load_dword v38, v253, s[20:23], s71 offen offset:1024 sc1\n"
"  buffer_load_dword v39, v253, s[20:23], s71 offen offset:2304 sc1\n"
"  buffer_load_dwordx4 v254, s[20:23], s71 offen sc1 lds\n"
"  buffer_load_dwordx4 v245, s[20:23], s71 offen offset:1024 sc1 lds\n"
"  s_branch .Lfb_oc1\n"
".Lfb_or2:\n"
"  s_add_u32 s58, s58, 1\n"
"  s_cmp_gt_u32 s58, 0x20000\n"
"  s_cbranch_scc1 .Lfb_ok2\n"
"  s_add_u32 m0, s84, 16384\n"
"  buffer_load_dword v36, v253, s[20:23], s70 offen offset:1024 sc1\n"
"  buffer_load_dword v37, v253, s[20:23], s70 offen offset:2304 sc1\n"
"  buffer_load_dwordx4 v254, s[20:23], s70 offen sc1 lds\n"
"  buffer_load_dwordx4 v245, s[20:23], s70 offen offset:1024 sc1 lds\n"
"  s_add_u32 m0, s84, 24576\n"
"  buffer_load_dword v38, v253, s[20:23], s71 offen offset:1024 sc1\n"
"  buffer_load_dword v39, v253, s[20:23], s71 offen offset:2304 sc1\n"
"  buffer_load_dwordx4 v254, s[20:23], s71 offen sc1 lds\n"
"  buffer_load_dwordx4 v245, s[20:23], s71 offen offset:1024 sc1 lds\n"
"  s_branch .Lfb_oc2\n"
".Lfb_or3:\n"
"  s_add_u32 s58, s58, 1\n"
"  s_cmp_gt_u32 s58, 0x20000\n"
"  s_cbranch_scc1 .Lfb_ok3\n"
"  s_add_u32 m0, s84, 24576\n"
"  buffer_load_dword v38, v253, s[20:23], s71 offen offset:1024 sc1\n"
"  buffer_load_dword v39, v253, s[20:23], s71 offen offset:2304 sc1\n"
"  buffer_load_dwordx4 v254, s[20:23], s71 offen sc1 lds\n"
"  buffer_load_dwordx4 v245, s[20:23], s71 offen offset:1024 sc1 lds\n"
"  s_branch .Lfb_oc3\n"
".Lfb_L_str:\n"
"  s_add_u32 s58, s58, 1\n"
"  s_cmp_gt_u32 s58, 0x20000\n"
"  s_cbranch_scc1 .Lfb_L_stk\n"
"  buffer_load_dwordx4 v[212:215], v254, s[20:23], s80 offen sc1\n"
"  buffer_load_dwordx4 v[216:219], v254, s[20:23], s80 offen offset:1024 sc1\n"
"  s_waitcnt vmcnt(0)\n"
"  s_branch .Lfb_L_stc\n"
".Lfb_L_car:\n"
"  s_add_u32 s58, s58, 1\n"
"  s_cmp_gt_u32 s58, 0x20000\n"
"  s_cbranch_scc1 .Lfb_L_cak\n"
"  buffer_load_dwordx2 v[168:169], v177, s[20:23], s84 offen offset:256 sc1\n"
"  buffer_load_dwordx2 v[170:171], v177, s[20:23], s85 offen offset:256 sc1\n"
"  buffer_load_dwordx4 v[160:163], v176, s[20:23], s84 offen sc1\n"
"  buffer_load_dwordx4 v[164:167], v176, s[20:23], s85 offen sc1\n"
"  s_waitcnt vmcnt(0)\n"
"  s_branch .Lfb_L_cac\n"
".Lfb_L_acr:\n"
"  s_add_u32 s58, s58, 1\n"
"  s_cmp_gt_u32 s58, 0x20000\n"
"  s_cbranch_scc1 .Lfb_L_ack\n"
"  buffer_load_dwordx4 v[116:119], v112, s[20:23], s75 offen sc1\n"
"  s_waitcnt vmcnt(0)\n"
"  s_branch .Lfb_L_acc\n"
".Lfb_M_str:\n"
"  s_add_u32 s58, s58, 1\n"
"  s_cmp_gt_u32 s58, 0x20000\n"
"  s_cbranch_scc1 .Lfb_M_stk\n"
"  buffer_load_dwordx4 v[212:215], v254, s[20:23], s80 offen sc1\n"
"  buffer_load_dwordx4 v[216:219], v254, s[20:23], s80 offen offset:1024 sc1\n"
"  s_waitcnt vmcnt(0)\n"
"  s_branch .Lfb_M_stc\n"
".Lfb_M_keyr:\n"
"  s_add_u32 s58, s58, 1\n"
"  s_cmp_gt_u32 s58, 0x20000\n"
"  s_cbranch_scc1 .Lfb_M_keyk\n"
"  s_waitcnt vmcnt(0)\n"
"  s_branch .Lfb_M_keys\n"
".Lfb_M_car:\n"
"  s_add_u32 s58, s58, 1\n"
"  s_cmp_gt_u32 s58, 0x20000\n"
"  s_cbranch_scc1 .Lfb_M_cak\n"
"  buffer_load_dwordx2 v[168:169], v177, s[20:23], s84 offen offset:256 sc1\n"
"  buffer_load_dwordx2 v[170:171], v177, s[20:23], s85 offen offset:256 sc1\n"
"  buffer_load_dwordx4 v[160:163], v176, s[20:23], s84 offen sc1\n"
"  buffer_load_dwordx4 v[164:167], v176, s[20:23], s85 offen sc1\n"
"  s_waitcnt vmcnt(0)\n"
"  s_branch .Lfb_M_cac\n"
".Lfb_M_acr:\n"
"  s_add_u32 s58, s58, 1\n"
"  s_cmp_gt_u32 s58, 0x20000\n"
"  s_cbranch_scc1 .Lfb_M_ack\n"
"  buffer_load_dwordx4 v[116:119], v112, s[20:23], s75 offen sc1\n"
"  s_waitcnt vmcnt(0)\n"
"  s_branch .Lfb_M_acc\n"
".Lfb_end:\n"

      :
      : "{s[0:1]}"(__builtin_amdgcn_kernarg_segment_ptr()), "{s2}"(__builtin_amdgcn_workgroup_id_x()), "{v0}"(__builtin_amdgcn_workitem_id_x()),
        "{s100}"((__attribute__((address_space(3))) char*)lds)
      : "v1", "v2", "v3", "v4", "v5", "v6", "v7", "v8", "v9", "v10", "v11", "v12", "v13", "v14", "v15", "v16", "v17", "v18", "v19", "v20", "v21", "v22", "v23", "v24", "v25", "v26", "v27", "v28", "v29", "v30", "v31", "v32", "v33", "v34", "v35", "v36", "v37", "v38", "v39", "v40", "v41", "v42", "v43", "v44", "v45", "v46", "v47", "v48", "v49", "v50", "v51", "v52", "v53", "v54", "v55", "v56", "v57", "v58", "v59", "v60", "v61", "v62", "v63", "v64", "v65", "v66", "v67", "v68", "v69", "v70", "v71", "v72", "v73", "v74", "v75", "v76", "v77", "v78", "v79", "v80", "v81", "v82", "v83", "v84", "v85", "v86", "v87", "v88", "v89", "v90", "v91", "v92", "v93", "v94", "v95", "v96", "v97", "v98", "v99", "v100", "v101", "v102", "v103", "v104", "v105", "v106", "v107", "v108", "v109", "v110", "v111", "v112", "v113", "v114", "v115", "v116", "v117", "v118", "v119", "v120", "v121", "v122", "v123", "v124", "v125", "v126", "v127", "v128", "v129", "v130", "v131", "v132", "v133", "v134", "v135", "v136", "v137", "v138", "v139", "v140", "v141", "v142", "v143", "v144", "v145", "v146", "v147", "v148", "v149", "v150", "v151", "v152", "v153", "v154", "v155", "v156", "v157", "v158", "v159", "v160", "v161", "v162", "v163", "v164", "v165", "v166", "v167", "v168", "v169", "v170", "v171", "v172", "v173", "v174", "v175", "v176", "v177", "v178", "v179", "v180", "v181", "v182", "v183", "v184", "v185", "v186", "v187", "v188", "v189", "v190", "v191", "v192", "v193", "v194", "v195", "v196", "v197", "v198", "v199", "v200", "v201", "v202", "v203", "v204", "v205", "v206", "v207", "v208", "v209", "v210", "v211", "v212", "v213", "v214", "v215", "v216", "v217", "v218", "v219", "v220", "v221", "v222", "v223", "v224", "v225", "v226", "v227", "v228", "v229", "v230", "v231", "v232", "v233", "v234", "v235", "v236", "v237", "v238", "v239", "v240", "v241", "v242", "v243", "v244", "v245", "v246", "v247", "v248", "v249", "v250", "v251", "v252", "v253", "v254", "v255", "s3", "s4", "s5", "s6", "s7", "s8", "s9", "s10", "s11", "s12", "s13", "s14", "s15", "s16", "s17", "s18", "s19", "s20", "s21", "s22", "s23", "s24", "s25", "s26", "s27", "s28", "s29", "s30", "s31", "s32", "s33", "s34", "s35", "s36", "s37", "s38", "s39", "s40", "s41", "s42", "s43", "s44", "s45", "s46", "s47", "s48", "s49", "s50", "s51", "s52", "s53", "s54", "s55", "s56", "s57", "s58", "s59", "s60", "s61", "s62", "s63", "s64", "s65", "s66", "s67", "s68", "s69", "s70", "s71", "s72", "s73", "s74", "s75", "s76", "s77", "s78", "s79", "s80", "s81", "s82", "s83", "s84", "s85", "s86", "s87", "s88", "s89", "s90", "s91", "s92", "s93", "s94", "s95", "s96", "s97", "s98", "s99", "vcc", "scc", "memory");
#endif
}

extern "C" void kernel_launch(void* const* d_in, const int* in_sizes, int n_in, void* d_out, int out_size, void* d_ws, size_t ws_size,
                              hipStream_t stream) {
  const u32 F = 0x00020000u;
  fused_block<<<256, 512, 0, stream>>>(d_in[0], 2048u * 1024u, F, d_in[1], 512u * 3072u, F, d_in[2], 512u * 1024u, F, d_ws, 16u << 20, F,
                                       d_in[5], 512u * 3584u, F, d_in[6], 512u * 3584u, F, d_in[7], 1792u * 1024u, F, d_out, 2048u * 1024u, F,
                                       d_in[4], 1024u, F);
}
```
